```python
import torch

def _softmax(x, axis=-1):
    return torch.softmax(x, dim=axis)
def _gelu(x):
    return torch.nn.functional.gelu(x, approximate="tanh")
def _silu(x):
    return torch.nn.functional.silu(x)
def _sigmoid(x):
    return torch.sigmoid(x)
def _layernorm(x, axis=-1, eps=1e-5):
    m = x.mean(dim=axis, keepdim=True)
    v = x.var(dim=axis, keepdim=True, unbiased=False)
    return (x - m) / torch.sqrt(v + eps)
def _scatter_add(tgt, idx, src, axis=0):
    return tgt.clone().index_add_(axis, idx, src)
def _segment_sum(src, seg, n):
    out = torch.zeros((n,) + src.shape[1:], dtype=src.dtype)
    return out.index_add_(0, seg, src)
def _pad(x, pw):
    flat = tuple(v for pair in reversed(pw) for v in pair)
    return torch.nn.functional.pad(x, flat)

kernel_name = 'standalone__detection_seg_fpn_lateral__vision'

def setup_inputs():

    _w = (lambda a: (lambda t: t.to(torch.bfloat16) if t.is_floating_point() else t)(torch.as_tensor(a)))
    H = W = (224 // 2) * 2
    C = 96
    k = 3
    return {
        "c_fine": _w((lambda *s: torch.randn(*s, dtype=torch.bfloat16) * 0.05)(1, H, W, C)),
        "p_coarse": _w((lambda *s: torch.randn(*s, dtype=torch.bfloat16) * 0.05)(1, H // 2, W // 2, C)),
        "w_lat": _w((lambda *s: torch.randn(*s, dtype=torch.bfloat16) * 0.05)(C, C)),
        "w_out": _w((lambda *s: torch.randn(*s, dtype=torch.bfloat16) * 0.05)(k, k, C, C)),
    }

def reference(c_fine, p_coarse, w_lat, w_out):
    B, H, W, C = c_fine.shape
    k = 3
    lat = torch.einsum("bhwc,cd->bhwd", c_fine, w_lat)
    up = (lambda x, n, axis: torch.repeat_interleave(x, n, dim=axis))((lambda x, n, axis: torch.repeat_interleave(x, n, dim=axis))(p_coarse, 2, axis=1), 2, axis=2)
    m = lat + up
    pad = k // 2
    mp = _pad(m, ((0, 0), (pad, pad), (pad, pad), (0, 0)))
    o = None
    for di in range(k):
        for dj in range(k):
            t = torch.einsum("bhwc,cd->bhwd", mp[:, di:di + H, dj:dj + W, :], w_out[di, dj])
            o = t if o is None else o + t
    return o
```

```cpp
#include <hip/hip_runtime.h>
#include <hip/hip_bf16.h>
#include <stdint.h>

typedef __attribute__((ext_vector_type(8))) short bf16x8;
typedef __attribute__((ext_vector_type(4))) short s16x4;
typedef __attribute__((ext_vector_type(4))) float f32x4;
typedef __attribute__((ext_vector_type(4))) unsigned int u32x4;
typedef __attribute__((ext_vector_type(2))) unsigned int u32x2;
typedef __attribute__((ext_vector_type(2))) __bf16 bf16x2_t;
typedef __attribute__((ext_vector_type(2))) float f32x2;

#define LDS_AS __attribute__((address_space(3)))

constexpr int H = 224, W = 224, C = 96;
constexpr int TS = 14;
constexpr int PLANE = 4096;
constexpr int M_BYTES = 12 * PLANE + 64;
constexpr int W_OFF = 12 * PLANE + 256;
constexpr int TAP_BYTES = 96 * 96 * 2;
constexpr int LDS_BYTES = W_OFF + 5 * TAP_BYTES;

__device__ __forceinline__ unsigned pack_bf16(float a, float b) {
  f32x2 v = {a, b};
  bf16x2_t r = __builtin_convertvector(v, bf16x2_t);
  return __builtin_bit_cast(unsigned, r);
}

__device__ __forceinline__ bf16x8 tr_frag(const char LDS_AS* p0, const char LDS_AS* p1) {
  s16x4 a = __builtin_amdgcn_ds_read_tr16_b64_v4i16((LDS_AS s16x4*)p0);
  s16x4 b = __builtin_amdgcn_ds_read_tr16_b64_v4i16((LDS_AS s16x4*)p1);
  bf16x8 r;
  r[0] = a[0]; r[1] = a[1]; r[2] = a[2]; r[3] = a[3];
  r[4] = b[0]; r[5] = b[1]; r[6] = b[2]; r[7] = b[3];
  return r;
}

__global__ __launch_bounds__(256, 1) void fpn_fused(const __hip_bfloat16* __restrict__ c_fine,
                                                    const __hip_bfloat16* __restrict__ p_coarse,
                                                    const __hip_bfloat16* __restrict__ w_lat,
                                                    const __hip_bfloat16* __restrict__ w_out,
                                                    __hip_bfloat16* __restrict__ out) {
  extern __shared__ __attribute__((aligned(16))) char smem_raw[];
  char LDS_AS* smem = (char LDS_AS*)smem_raw;
  const int tid = threadIdx.x;
  const int lane = tid & 63;
  const int wave = __builtin_amdgcn_readfirstlane(tid >> 6);
  const int p = lane & 15, q = lane >> 4;
  const int bid = blockIdx.x;
  const int xcd = bid & 7, idx = bid >> 3;
  const int ty = (xcd >> 1) * 4 + (idx >> 3);
  const int tx = (xcd & 1) * 8 + (idx & 7);
  const int y0 = ty * TS, x0 = tx * TS;

  auto rs_c = __builtin_amdgcn_make_buffer_rsrc((void*)c_fine, 0, H * W * C * 2, 0x00020000);
  auto rs_p = __builtin_amdgcn_make_buffer_rsrc((void*)p_coarse, 0, (H / 2) * (W / 2) * C * 2, 0x00020000);
  auto rs_o = __builtin_amdgcn_make_buffer_rsrc((void*)out, 0, H * W * C * 2, 0x00020000);

  u32x4 cf[4][3], up[4][3];
  {
    const int gx = x0 - 1 + p;
    const bool xin = (gx >= 0) && (gx < W);
#pragma unroll
    for (int r = 0; r < 4; ++r) {
      const int gy = y0 - 1 + wave * 4 + r;
      const bool in = xin && (gy >= 0) && (gy < H);
      const unsigned oc = in ? (unsigned)((gy * W + gx) * 192 + q * 16) : 0x80000000u;
      const unsigned op = in ? (unsigned)(((gy >> 1) * (W / 2) + (gx >> 1)) * 192 + q * 16) : 0x80000000u;
#pragma unroll
      for (int ks = 0; ks < 3; ++ks) {
        cf[r][ks] = __builtin_amdgcn_raw_buffer_load_b128(rs_c, oc + ks * 64, 0, 0);
        up[r][ks] = __builtin_amdgcn_raw_buffer_load_b128(rs_p, op + ks * 64, 0, 0);
      }
    }
  }
  {
    for (int i = wave; i < 18; i += 4)
      __builtin_amdgcn_global_load_lds((const char*)w_lat + i * 1024 + lane * 16,
                                       (LDS_AS void*)(smem + W_OFF + i * 1024), 16, 0, 0);
  }
  auto stage_tap = [&](int tap, int slot) {
    for (int i = wave; i < 18; i += 4) {
      const int g = i * 64 + lane;
      const int row = g / 12, gr = g - row * 12;
      const int sgr = gr ^ (((row >> 3) & 1) << 1);
      __builtin_amdgcn_global_load_lds((const char*)w_out + tap * TAP_BYTES + row * 192 + sgr * 16,
                                       (LDS_AS void*)(smem + W_OFF + slot * TAP_BYTES + i * 1024), 16, 0, 0);
    }
  };
  for (int t = 0; t < 4; ++t) stage_tap(t, t + 1);
  __syncthreads();

  {
    bf16x8 wl[3][6];
    const int qq = (lane & 15) >> 2, pp = lane & 3;
#pragma unroll
    for (int ks = 0; ks < 3; ++ks)
#pragma unroll
      for (int t = 0; t < 6; ++t) {
        const int pair = t >> 1, h = t & 1;
        const char LDS_AS* b = smem + W_OFF + (32 * ks + 8 * q + qq) * 192 + (32 * pair + 8 * pp + 4 * h) * 2;
        wl[ks][t] = tr_frag(b, b + 4 * 192);
      }
#pragma unroll
    for (int r = 0; r < 4; ++r) {
      f32x4 acc[6];
#pragma unroll
      for (int t = 0; t < 6; ++t) {
        const int pair = t >> 1, h = t & 1;
        const unsigned u0 = up[r][pair][2 * h], u1 = up[r][pair][2 * h + 1];
        acc[t][0] = __builtin_bit_cast(float, u0 << 16);
        acc[t][1] = __builtin_bit_cast(float, u0 & 0xffff0000u);
        acc[t][2] = __builtin_bit_cast(float, u1 << 16);
        acc[t][3] = __builtin_bit_cast(float, u1 & 0xffff0000u);
      }
#pragma unroll
      for (int ks = 0; ks < 3; ++ks)
#pragma unroll
        for (int t = 0; t < 6; ++t)
          acc[t] = __builtin_amdgcn_mfma_f32_16x16x32_bf16(wl[ks][t], __builtin_bit_cast(bf16x8, cf[r][ks]), acc[t], 0, 0, 0);
#pragma unroll
      for (int pair = 0; pair < 3; ++pair) {
        u32x4 v;
        v[0] = pack_bf16(acc[2 * pair][0], acc[2 * pair][1]);
        v[1] = pack_bf16(acc[2 * pair][2], acc[2 * pair][3]);
        v[2] = pack_bf16(acc[2 * pair + 1][0], acc[2 * pair + 1][1]);
        v[3] = pack_bf16(acc[2 * pair + 1][2], acc[2 * pair + 1][3]);
        *(LDS_AS u32x4*)(smem + (4 * pair + q) * PLANE + ((wave * 4 + r) * 16 + p) * 16) = v;
      }
    }
  }

  const int nh = wave & 1, mh = wave >> 1;
  bf16x8 wo[9][3][3];
  {
    const int qq = (lane & 15) >> 2, pp = lane & 3;
    auto load_tap = [&](int tap, int slot) {
#pragma unroll
      for (int ks = 0; ks < 3; ++ks)
#pragma unroll
        for (int t = 0; t < 3; ++t) {
          const int colb = (48 * nh + 16 * t + 4 * pp) * 2;
          const int gr = colb >> 4;
          const int sgr = gr ^ ((q & 1) << 1);
          const char LDS_AS* b = smem + W_OFF + slot * TAP_BYTES + (32 * ks + 8 * q + qq) * 192 + sgr * 16 + (colb & 15);
          wo[tap][ks][t] = tr_frag(b, b + 4 * 192);
        }
    };
#pragma unroll
    for (int t = 0; t < 4; ++t) load_tap(t, t + 1);
    __syncthreads();
#pragma unroll
    for (int t = 4; t < 9; ++t) stage_tap(t, t - 4);
    __syncthreads();
#pragma unroll
    for (int t = 4; t < 9; ++t) load_tap(t, t - 4);
  }

  for (int it = 0; it < 7; ++it) {
    const int o = mh * 7 + it;
    f32x4 acc[3] = {{0, 0, 0, 0}, {0, 0, 0, 0}, {0, 0, 0, 0}};
    const char LDS_AS* mb = smem + q * PLANE + (o * 16 + p) * 16;
#pragma unroll
    for (int tap = 0; tap < 9; ++tap) {
      const int dy = tap / 3, dx = tap % 3;
#pragma unroll
      for (int ks = 0; ks < 3; ++ks) {
        bf16x8 b = *(const LDS_AS bf16x8*)(mb + ks * 4 * PLANE + (dy * 16 + dx) * 16);
#pragma unroll
        for (int t = 0; t < 3; ++t)
          acc[t] = __builtin_amdgcn_mfma_f32_16x16x32_bf16(wo[tap][ks][t], b, acc[t], 0, 0, 0);
      }
    }
    const unsigned oo = (p < TS) ? (unsigned)(((y0 + o) * W + x0 + p) * 192 + nh * 96 + q * 8) : 0x80000000u;
#pragma unroll
    for (int t = 0; t < 3; ++t) {
      u32x2 v;
      v[0] = pack_bf16(acc[t][0], acc[t][1]);
      v[1] = pack_bf16(acc[t][2], acc[t][3]);
      __builtin_amdgcn_raw_buffer_store_b64(v, rs_o, oo + t * 32, 0, 0);
    }
  }
}

extern "C" void kernel_launch(void* const* d_in, const int* in_sizes, int n_in,
                              void* d_out, int out_size, void* d_ws, size_t ws_size,
                              hipStream_t stream) {
  const __hip_bfloat16* c_fine = (const __hip_bfloat16*)d_in[0];
  const __hip_bfloat16* p_coarse = (const __hip_bfloat16*)d_in[1];
  const __hip_bfloat16* w_lat = (const __hip_bfloat16*)d_in[2];
  const __hip_bfloat16* w_out = (const __hip_bfloat16*)d_in[3];
  __hip_bfloat16* out = (__hip_bfloat16*)d_out;
  (void)hipFuncSetAttribute((const void*)fpn_fused, hipFuncAttributeMaxDynamicSharedMemorySize, LDS_BYTES);
  fpn_fused<<<256, 256, LDS_BYTES, stream>>>(c_fine, p_coarse, w_lat, w_out, out);
}
```

```cpp
#define AMP_EXPR 0ull
#include <hip/hip_runtime.h>
#include <hip/hip_bf16.h>
#include <stdint.h>

typedef __attribute__((ext_vector_type(8))) short bf16x8;
typedef __attribute__((ext_vector_type(4))) short s16x4;
typedef __attribute__((ext_vector_type(4))) float f32x4;
typedef __attribute__((ext_vector_type(4))) unsigned int u32x4;
typedef __attribute__((ext_vector_type(2))) unsigned int u32x2;
typedef __attribute__((ext_vector_type(2))) __bf16 bf16x2_t;
typedef __attribute__((ext_vector_type(2))) float f32x2;

#define STAMPS 1
#define NSTAMP 10
#define NWAVES 4
#define LDS_AS __attribute__((address_space(3)))

constexpr int H = 224, W = 224, C = 96;
constexpr int TS = 14;
constexpr int PLANE = 4096;
constexpr int M_BYTES = 12 * PLANE + 64;
constexpr int W_OFF = 12 * PLANE + 256;
constexpr int TAP_BYTES = 96 * 96 * 2;
constexpr int LDS_BYTES = W_OFF + 5 * TAP_BYTES;

__device__ __forceinline__ unsigned pack_bf16(float a, float b) {
  f32x2 v = {a, b};
  bf16x2_t r = __builtin_convertvector(v, bf16x2_t);
  return __builtin_bit_cast(unsigned, r);
}

__device__ __forceinline__ bf16x8 tr_frag(const char LDS_AS* p0, const char LDS_AS* p1) {
  s16x4 a = __builtin_amdgcn_ds_read_tr16_b64_v4i16((LDS_AS s16x4*)p0);
  s16x4 b = __builtin_amdgcn_ds_read_tr16_b64_v4i16((LDS_AS s16x4*)p1);
  bf16x8 r;
  r[0] = a[0]; r[1] = a[1]; r[2] = a[2]; r[3] = a[3];
  r[4] = b[0]; r[5] = b[1]; r[6] = b[2]; r[7] = b[3];
  return r;
}

__global__ __launch_bounds__(256, 1) void fpn_fused(const __hip_bfloat16* __restrict__ c_fine,
                                                    const __hip_bfloat16* __restrict__ p_coarse,
                                                    const __hip_bfloat16* __restrict__ w_lat,
                                                    const __hip_bfloat16* __restrict__ w_out,
                                                    __hip_bfloat16* __restrict__ out, unsigned long long* __restrict__ stamps) {
  unsigned long long ts[NSTAMP];
#define STAMP(i) do { __builtin_amdgcn_sched_barrier(0); asm volatile("s_memtime %0\n\ts_waitcnt lgkmcnt(0)" : "=s"(ts[i]) :: "memory"); __builtin_amdgcn_sched_barrier(0);} while(0)
  asm volatile("s_memrealtime %0\n\ts_waitcnt lgkmcnt(0)" : "=s"(ts[0]) :: "memory");
  STAMP(1);
  extern __shared__ __attribute__((aligned(16))) char smem_raw[];
  char LDS_AS* smem = (char LDS_AS*)smem_raw;
  const int tid = threadIdx.x;
  const int lane = tid & 63;
  const int wave = __builtin_amdgcn_readfirstlane(tid >> 6);
  const int p = lane & 15, q = lane >> 4;
  const int bid = blockIdx.x;
  const int xcd = bid & 7, idx = bid >> 3;
  const int ty = (xcd >> 1) * 4 + (idx >> 3);
  const int tx = (xcd & 1) * 8 + (idx & 7);
  const int y0 = ty * TS, x0 = tx * TS;

  auto rs_c = __builtin_amdgcn_make_buffer_rsrc((void*)c_fine, 0, H * W * C * 2, 0x00020000);
  auto rs_p = __builtin_amdgcn_make_buffer_rsrc((void*)p_coarse, 0, (H / 2) * (W / 2) * C * 2, 0x00020000);
  auto rs_o = __builtin_amdgcn_make_buffer_rsrc((void*)out, 0, H * W * C * 2, 0x00020000);

  u32x4 cf[4][3], up[4][3];
  {
    const int gx = x0 - 1 + p;
    const bool xin = (gx >= 0) && (gx < W);
#pragma unroll
    for (int r = 0; r < 4; ++r) {
      const int gy = y0 - 1 + wave * 4 + r;
      const bool in = xin && (gy >= 0) && (gy < H);
      const unsigned oc = in ? (unsigned)((gy * W + gx) * 192 + q * 16) : 0x80000000u;
      const unsigned op = in ? (unsigned)(((gy >> 1) * (W / 2) + (gx >> 1)) * 192 + q * 16) : 0x80000000u;
#pragma unroll
      for (int ks = 0; ks < 3; ++ks) {
        cf[r][ks] = __builtin_amdgcn_raw_buffer_load_b128(rs_c, oc + ks * 64, 0, 0);
        up[r][ks] = __builtin_amdgcn_raw_buffer_load_b128(rs_p, op + ks * 64, 0, 0);
      }
    }
  }
  {
    for (int i = wave; i < 18; i += 4)
      __builtin_amdgcn_global_load_lds((const char*)w_lat + i * 1024 + lane * 16,
                                       (LDS_AS void*)(smem + W_OFF + i * 1024), 16, 0, 0);
  }
  auto stage_tap = [&](int tap, int slot) {
    for (int i = wave; i < 18; i += 4) {
      const int g = i * 64 + lane;
      const int row = g / 12, gr = g - row * 12;
      const int sgr = gr ^ (((row >> 3) & 1) << 1);
      __builtin_amdgcn_global_load_lds((const char*)w_out + tap * TAP_BYTES + row * 192 + sgr * 16,
                                       (LDS_AS void*)(smem + W_OFF + slot * TAP_BYTES + i * 1024), 16, 0, 0);
    }
  };
  for (int t = 0; t < 4; ++t) stage_tap(t, t + 1);
  STAMP(2);
  __syncthreads();
  STAMP(3);

  {
    bf16x8 wl[3][6];
    const int qq = (lane & 15) >> 2, pp = lane & 3;
#pragma unroll
    for (int ks = 0; ks < 3; ++ks)
#pragma unroll
      for (int t = 0; t < 6; ++t) {
        const int pair = t >> 1, h = t & 1;
        const char LDS_AS* b = smem + W_OFF + (32 * ks + 8 * q + qq) * 192 + (32 * pair + 8 * pp + 4 * h) * 2;
        wl[ks][t] = tr_frag(b, b + 4 * 192);
      }
#pragma unroll
    for (int r = 0; r < 4; ++r) {
      f32x4 acc[6];
#pragma unroll
      for (int t = 0; t < 6; ++t) {
        const int pair = t >> 1, h = t & 1;
        const unsigned u0 = up[r][pair][2 * h], u1 = up[r][pair][2 * h + 1];
        acc[t][0] = __builtin_bit_cast(float, u0 << 16);
        acc[t][1] = __builtin_bit_cast(float, u0 & 0xffff0000u);
        acc[t][2] = __builtin_bit_cast(float, u1 << 16);
        acc[t][3] = __builtin_bit_cast(float, u1 & 0xffff0000u);
      }
#pragma unroll
      for (int ks = 0; ks < 3; ++ks)
#pragma unroll
        for (int t = 0; t < 6; ++t)
          acc[t] = __builtin_amdgcn_mfma_f32_16x16x32_bf16(wl[ks][t], __builtin_bit_cast(bf16x8, cf[r][ks]), acc[t], 0, 0, 0);
#pragma unroll
      for (int pair = 0; pair < 3; ++pair) {
        u32x4 v;
        v[0] = pack_bf16(acc[2 * pair][0], acc[2 * pair][1]);
        v[1] = pack_bf16(acc[2 * pair][2], acc[2 * pair][3]);
        v[2] = pack_bf16(acc[2 * pair + 1][0], acc[2 * pair + 1][1]);
        v[3] = pack_bf16(acc[2 * pair + 1][2], acc[2 * pair + 1][3]);
        *(LDS_AS u32x4*)(smem + (4 * pair + q) * PLANE + ((wave * 4 + r) * 16 + p) * 16) = v;
      }
    }
  }

  STAMP(4);
  const int nh = wave & 1, mh = wave >> 1;
  bf16x8 wo[9][3][3];
  {
    const int qq = (lane & 15) >> 2, pp = lane & 3;
    auto load_tap = [&](int tap, int slot) {
#pragma unroll
      for (int ks = 0; ks < 3; ++ks)
#pragma unroll
        for (int t = 0; t < 3; ++t) {
          const int colb = (48 * nh + 16 * t + 4 * pp) * 2;
          const int gr = colb >> 4;
          const int sgr = gr ^ ((q & 1) << 1);
          const char LDS_AS* b = smem + W_OFF + slot * TAP_BYTES + (32 * ks + 8 * q + qq) * 192 + sgr * 16 + (colb & 15);
          wo[tap][ks][t] = tr_frag(b, b + 4 * 192);
        }
    };
#pragma unroll
    for (int t = 0; t < 4; ++t) load_tap(t, t + 1);
    __syncthreads();
    STAMP(5);
#pragma unroll
    for (int t = 4; t < 9; ++t) stage_tap(t, t - 4);
    __syncthreads();
    STAMP(6);
#pragma unroll
    for (int t = 4; t < 9; ++t) load_tap(t, t - 4);
  }

  STAMP(7);
  for (int it = 0; it < 7; ++it) {
    const int o = mh * 7 + it;
    f32x4 acc[3] = {{0, 0, 0, 0}, {0, 0, 0, 0}, {0, 0, 0, 0}};
    const char LDS_AS* mb = smem + q * PLANE + (o * 16 + p) * 16;
#pragma unroll
    for (int tap = 0; tap < 9; ++tap) {
      const int dy = tap / 3, dx = tap % 3;
#pragma unroll
      for (int ks = 0; ks < 3; ++ks) {
        bf16x8 b = *(const LDS_AS bf16x8*)(mb + ks * 4 * PLANE + (dy * 16 + dx) * 16);
#pragma unroll
        for (int t = 0; t < 3; ++t)
          acc[t] = __builtin_amdgcn_mfma_f32_16x16x32_bf16(wo[tap][ks][t], b, acc[t], 0, 0, 0);
      }
    }
    const unsigned oo = (p < TS) ? (unsigned)(((y0 + o) * W + x0 + p) * 192 + nh * 96 + q * 8) : 0x80000000u;
#pragma unroll
    for (int t = 0; t < 3; ++t) {
      u32x2 v;
      v[0] = pack_bf16(acc[t][0], acc[t][1]);
      v[1] = pack_bf16(acc[t][2], acc[t][3]);
      __builtin_amdgcn_raw_buffer_store_b64(v, rs_o, oo + t * 32, 0, 0);
    }
  }
  STAMP(8);
  asm volatile("s_waitcnt vmcnt(0)" ::: "memory");
  STAMP(9);
  {
    unsigned long long extra = AMP_EXPR;
    unsigned long long tend = ts[9] + extra, now;
    do { asm volatile("s_sleep 2\n\ts_memtime %0\n\ts_waitcnt lgkmcnt(0)" : "=s"(now) :: "memory"); } while (now < tend);
  }
}

extern "C" void kernel_launch(void* const* d_in, const int* in_sizes, int n_in,
                              void* d_out, int out_size, void* d_ws, size_t ws_size,
                              hipStream_t stream) {
  const __hip_bfloat16* c_fine = (const __hip_bfloat16*)d_in[0];
  const __hip_bfloat16* p_coarse = (const __hip_bfloat16*)d_in[1];
  const __hip_bfloat16* w_lat = (const __hip_bfloat16*)d_in[2];
  const __hip_bfloat16* w_out = (const __hip_bfloat16*)d_in[3];
  __hip_bfloat16* out = (__hip_bfloat16*)d_out;
  (void)hipFuncSetAttribute((const void*)fpn_fused, hipFuncAttributeMaxDynamicSharedMemorySize, LDS_BYTES);
  fpn_fused<<<256, 256, LDS_BYTES, stream>>>(c_fine, p_coarse, w_lat, w_out, out, (unsigned long long*)d_ws);
}
```

```cpp
#define AMP_CYC 0ull
#define AMP_RT (rt[3]-rt[1])
#include <hip/hip_runtime.h>
#include <hip/hip_bf16.h>
#include <stdint.h>

typedef __attribute__((ext_vector_type(8))) short bf16x8;
typedef __attribute__((ext_vector_type(4))) short s16x4;
typedef __attribute__((ext_vector_type(4))) float f32x4;
typedef __attribute__((ext_vector_type(4))) unsigned int u32x4;
typedef __attribute__((ext_vector_type(2))) unsigned int u32x2;
typedef __attribute__((ext_vector_type(2))) __bf16 bf16x2_t;
typedef __attribute__((ext_vector_type(2))) float f32x2;

#define STAMPS 1
#define NSTAMP 10
#define NWAVES 4
#define LDS_AS __attribute__((address_space(3)))

constexpr int H = 224, W = 224, C = 96;
constexpr int TS = 14;
constexpr int PLANE = 4096;
constexpr int M_BYTES = 12 * PLANE + 64;
constexpr int W_OFF = 12 * PLANE + 256;
constexpr int TAP_BYTES = 96 * 96 * 2;
constexpr int LDS_BYTES = W_OFF + 5 * TAP_BYTES;

__device__ __forceinline__ unsigned pack_bf16(float a, float b) {
  f32x2 v = {a, b};
  bf16x2_t r = __builtin_convertvector(v, bf16x2_t);
  return __builtin_bit_cast(unsigned, r);
}

__device__ __forceinline__ bf16x8 tr_frag(const char LDS_AS* p0, const char LDS_AS* p1) {
  s16x4 a = __builtin_amdgcn_ds_read_tr16_b64_v4i16((LDS_AS s16x4*)p0);
  s16x4 b = __builtin_amdgcn_ds_read_tr16_b64_v4i16((LDS_AS s16x4*)p1);
  bf16x8 r;
  r[0] = a[0]; r[1] = a[1]; r[2] = a[2]; r[3] = a[3];
  r[4] = b[0]; r[5] = b[1]; r[6] = b[2]; r[7] = b[3];
  return r;
}

__global__ __launch_bounds__(256, 1) void fpn_fused(const __hip_bfloat16* __restrict__ c_fine,
                                                    const __hip_bfloat16* __restrict__ p_coarse,
                                                    const __hip_bfloat16* __restrict__ w_lat,
                                                    const __hip_bfloat16* __restrict__ w_out,
                                                    __hip_bfloat16* __restrict__ out, unsigned long long* __restrict__ stamps) {
  unsigned long long ts[NSTAMP], rt[NSTAMP];
#define STAMP(i) do { __builtin_amdgcn_sched_barrier(0); asm volatile("s_memtime %0\n\ts_memrealtime %1\n\ts_waitcnt lgkmcnt(0)" : "=s"(ts[i]), "=s"(rt[i]) :: "memory"); __builtin_amdgcn_sched_barrier(0);} while(0)
  asm volatile("s_memrealtime %0\n\ts_waitcnt lgkmcnt(0)" : "=s"(ts[0]) :: "memory");
  STAMP(1);

  extern __shared__ __attribute__((aligned(16))) char smem_raw[];
  char LDS_AS* smem = (char LDS_AS*)smem_raw;
  const int tid = threadIdx.x;
  const int lane = tid & 63;
  const int wave = __builtin_amdgcn_readfirstlane(tid >> 6);
  const int p = lane & 15, q = lane >> 4;
  const int bid = blockIdx.x;
  const int xcd = bid & 7, idx = bid >> 3;
  const int ty = (xcd >> 1) * 4 + (idx >> 3);
  const int tx = (xcd & 1) * 8 + (idx & 7);
  const int y0 = ty * TS, x0 = tx * TS;

  auto rs_c = __builtin_amdgcn_make_buffer_rsrc((void*)c_fine, 0, H * W * C * 2, 0x00020000);
  auto rs_p = __builtin_amdgcn_make_buffer_rsrc((void*)p_coarse, 0, (H / 2) * (W / 2) * C * 2, 0x00020000);
  auto rs_o = __builtin_amdgcn_make_buffer_rsrc((void*)out, 0, H * W * C * 2, 0x00020000);

  u32x4 cf[4][3], up[4][3];
  {
    const int gx = x0 - 1 + p;
    const bool xin = (gx >= 0) && (gx < W);
#pragma unroll
    for (int r = 0; r < 4; ++r) {
      const int gy = y0 - 1 + wave * 4 + r;
      const bool in = xin && (gy >= 0) && (gy < H);
      const unsigned oc = in ? (unsigned)((gy * W + gx) * 192 + q * 16) : 0x80000000u;
      const unsigned op = in ? (unsigned)(((gy >> 1) * (W / 2) + (gx >> 1)) * 192 + q * 16) : 0x80000000u;
#pragma unroll
      for (int ks = 0; ks < 3; ++ks) {
        cf[r][ks] = __builtin_amdgcn_raw_buffer_load_b128(rs_c, oc + ks * 64, 0, 0);
        up[r][ks] = __builtin_amdgcn_raw_buffer_load_b128(rs_p, op + ks * 64, 0, 0);
      }
    }
  }
  {
    for (int i = wave; i < 18; i += 4)
      __builtin_amdgcn_global_load_lds((const char*)w_lat + i * 1024 + lane * 16,
                                       (LDS_AS void*)(smem + W_OFF + i * 1024), 16, 0, 0);
  }
  auto stage_tap = [&](int tap, int slot) {
    for (int i = wave; i < 18; i += 4) {
      const int g = i * 64 + lane;
      const int row = g / 12, gr = g - row * 12;
      const int sgr = gr ^ (((row >> 3) & 1) << 1);
      __builtin_amdgcn_global_load_lds((const char*)w_out + tap * TAP_BYTES + row * 192 + sgr * 16,
                                       (LDS_AS void*)(smem + W_OFF + slot * TAP_BYTES + i * 1024), 16, 0, 0);
    }
  };
  for (int t = 0; t < 4; ++t) stage_tap(t, t + 1);
  STAMP(2);
  __syncthreads();
  STAMP(3);

  {
    bf16x8 wl[3][6];
    const int qq = (lane & 15) >> 2, pp = lane & 3;
#pragma unroll
    for (int ks = 0; ks < 3; ++ks)
#pragma unroll
      for (int t = 0; t < 6; ++t) {
        const int pair = t >> 1, h = t & 1;
        const char LDS_AS* b = smem + W_OFF + (32 * ks + 8 * q + qq) * 192 + (32 * pair + 8 * pp + 4 * h) * 2;
        wl[ks][t] = tr_frag(b, b + 4 * 192);
      }
#pragma unroll
    for (int r = 0; r < 4; ++r) {
      f32x4 acc[6];
#pragma unroll
      for (int t = 0; t < 6; ++t) {
        const int pair = t >> 1, h = t & 1;
        const unsigned u0 = up[r][pair][2 * h], u1 = up[r][pair][2 * h + 1];
        acc[t][0] = __builtin_bit_cast(float, u0 << 16);
        acc[t][1] = __builtin_bit_cast(float, u0 & 0xffff0000u);
        acc[t][2] = __builtin_bit_cast(float, u1 << 16);
        acc[t][3] = __builtin_bit_cast(float, u1 & 0xffff0000u);
      }
#pragma unroll
      for (int ks = 0; ks < 3; ++ks)
#pragma unroll
        for (int t = 0; t < 6; ++t)
          acc[t] = __builtin_amdgcn_mfma_f32_16x16x32_bf16(wl[ks][t], __builtin_bit_cast(bf16x8, cf[r][ks]), acc[t], 0, 0, 0);
#pragma unroll
      for (int pair = 0; pair < 3; ++pair) {
        u32x4 v;
        v[0] = pack_bf16(acc[2 * pair][0], acc[2 * pair][1]);
        v[1] = pack_bf16(acc[2 * pair][2], acc[2 * pair][3]);
        v[2] = pack_bf16(acc[2 * pair + 1][0], acc[2 * pair + 1][1]);
        v[3] = pack_bf16(acc[2 * pair + 1][2], acc[2 * pair + 1][3]);
        *(LDS_AS u32x4*)(smem + (4 * pair + q) * PLANE + ((wave * 4 + r) * 16 + p) * 16) = v;
      }
    }
  }

  STAMP(4);
  const int nh = wave & 1, mh = wave >> 1;
  bf16x8 wo[9][3][3];
  {
    const int qq = (lane & 15) >> 2, pp = lane & 3;
    auto load_tap = [&](int tap, int slot) {
#pragma unroll
      for (int ks = 0; ks < 3; ++ks)
#pragma unroll
        for (int t = 0; t < 3; ++t) {
          const int colb = (48 * nh + 16 * t + 4 * pp) * 2;
          const int gr = colb >> 4;
          const int sgr = gr ^ ((q & 1) << 1);
          const char LDS_AS* b = smem + W_OFF + slot * TAP_BYTES + (32 * ks + 8 * q + qq) * 192 + sgr * 16 + (colb & 15);
          wo[tap][ks][t] = tr_frag(b, b + 4 * 192);
        }
    };
#pragma unroll
    for (int t = 0; t < 4; ++t) load_tap(t, t + 1);
    __syncthreads();
    STAMP(5);
#pragma unroll
    for (int t = 4; t < 9; ++t) stage_tap(t, t - 4);
    __syncthreads();
    STAMP(6);
#pragma unroll
    for (int t = 4; t < 9; ++t) load_tap(t, t - 4);
  }

  STAMP(7);
  for (int it = 0; it < 7; ++it) {
    const int o = mh * 7 + it;
    f32x4 acc[3] = {{0, 0, 0, 0}, {0, 0, 0, 0}, {0, 0, 0, 0}};
    const char LDS_AS* mb = smem + q * PLANE + (o * 16 + p) * 16;
#pragma unroll
    for (int tap = 0; tap < 9; ++tap) {
      const int dy = tap / 3, dx = tap % 3;
#pragma unroll
      for (int ks = 0; ks < 3; ++ks) {
        bf16x8 b = *(const LDS_AS bf16x8*)(mb + ks * 4 * PLANE + (dy * 16 + dx) * 16);
#pragma unroll
        for (int t = 0; t < 3; ++t)
          acc[t] = __builtin_amdgcn_mfma_f32_16x16x32_bf16(wo[tap][ks][t], b, acc[t], 0, 0, 0);
      }
    }
    const unsigned oo = (p < TS) ? (unsigned)(((y0 + o) * W + x0 + p) * 192 + nh * 96 + q * 8) : 0x80000000u;
#pragma unroll
    for (int t = 0; t < 3; ++t) {
      u32x2 v;
      v[0] = pack_bf16(acc[t][0], acc[t][1]);
      v[1] = pack_bf16(acc[t][2], acc[t][3]);
      __builtin_amdgcn_raw_buffer_store_b64(v, rs_o, oo + t * 32, 0, 0);
    }
  }
  STAMP(8);
  asm volatile("s_waitcnt vmcnt(0)" ::: "memory");
  STAMP(9);
  {
    unsigned long long tend = ts[9] + (AMP_CYC), now;
    do { asm volatile("s_sleep 2\n\ts_memtime %0\n\ts_waitcnt lgkmcnt(0)" : "=s"(now) :: "memory"); } while (now < tend);
    tend = rt[9] + (AMP_RT);
    do { asm volatile("s_sleep 2\n\ts_memrealtime %0\n\ts_waitcnt lgkmcnt(0)" : "=s"(now) :: "memory"); } while (now < tend);
  }
}

extern "C" void kernel_launch(void* const* d_in, const int* in_sizes, int n_in,
                              void* d_out, int out_size, void* d_ws, size_t ws_size,
                              hipStream_t stream) {
  const __hip_bfloat16* c_fine = (const __hip_bfloat16*)d_in[0];
  const __hip_bfloat16* p_coarse = (const __hip_bfloat16*)d_in[1];
  const __hip_bfloat16* w_lat = (const __hip_bfloat16*)d_in[2];
  const __hip_bfloat16* w_out = (const __hip_bfloat16*)d_in[3];
  __hip_bfloat16* out = (__hip_bfloat16*)d_out;
  (void)hipFuncSetAttribute((const void*)fpn_fused, hipFuncAttributeMaxDynamicSharedMemorySize, LDS_BYTES);
  fpn_fused<<<256, 256, LDS_BYTES, stream>>>(c_fine, p_coarse, w_lat, w_out, out, (unsigned long long*)d_ws);
}
```

```cpp
#include <hip/hip_runtime.h>
#include <hip/hip_bf16.h>
#include <stdint.h>

typedef __attribute__((ext_vector_type(8))) short bf16x8;
typedef __attribute__((ext_vector_type(4))) short s16x4;
typedef __attribute__((ext_vector_type(4))) float f32x4;
typedef __attribute__((ext_vector_type(4))) unsigned int u32x4;
typedef __attribute__((ext_vector_type(2))) unsigned int u32x2;
typedef __attribute__((ext_vector_type(2))) __bf16 bf16x2_t;
typedef __attribute__((ext_vector_type(2))) float f32x2;

#define LDS_AS __attribute__((address_space(3)))

constexpr int H = 224, W = 224, C = 96;
constexpr int TS = 14;
constexpr int PLANE = 4096;
constexpr int M_BYTES = 12 * PLANE + 64;
constexpr int W_OFF = 12 * PLANE + 256;
constexpr int TAP_BYTES = 96 * 96 * 2;
constexpr int LDS_BYTES = W_OFF + 5 * TAP_BYTES;

__device__ __forceinline__ unsigned pack_bf16(float a, float b) {
  f32x2 v = {a, b};
  bf16x2_t r = __builtin_convertvector(v, bf16x2_t);
  return __builtin_bit_cast(unsigned, r);
}

__device__ __forceinline__ bf16x8 tr_frag(const char LDS_AS* p0, const char LDS_AS* p1) {
  s16x4 a = __builtin_amdgcn_ds_read_tr16_b64_v4i16((LDS_AS s16x4*)p0);
  s16x4 b = __builtin_amdgcn_ds_read_tr16_b64_v4i16((LDS_AS s16x4*)p1);
  bf16x8 r;
  r[0] = a[0]; r[1] = a[1]; r[2] = a[2]; r[3] = a[3];
  r[4] = b[0]; r[5] = b[1]; r[6] = b[2]; r[7] = b[3];
  return r;
}

__global__ __launch_bounds__(256, 1) void fpn_fused(const __hip_bfloat16* __restrict__ c_fine,
                                                    const __hip_bfloat16* __restrict__ p_coarse,
                                                    const __hip_bfloat16* __restrict__ w_lat,
                                                    const __hip_bfloat16* __restrict__ w_out,
                                                    __hip_bfloat16* __restrict__ out) {
  extern __shared__ __attribute__((aligned(16))) char smem_raw[];
  char LDS_AS* smem = (char LDS_AS*)smem_raw;
  const int tid = threadIdx.x;
  const int lane = tid & 63;
  const int wave = __builtin_amdgcn_readfirstlane(tid >> 6);
  const int p = lane & 15, q = lane >> 4;
  const int bid = blockIdx.x;
  const int xcd = bid & 7, idx = bid >> 3;
  const int ty = (xcd >> 1) * 4 + (idx >> 3);
  const int tx = (xcd & 1) * 8 + (idx & 7);
  const int y0 = ty * TS, x0 = tx * TS;

  auto rs_c = __builtin_amdgcn_make_buffer_rsrc((void*)c_fine, 0, H * W * C * 2, 0x00020000);
  auto rs_p = __builtin_amdgcn_make_buffer_rsrc((void*)p_coarse, 0, (H / 2) * (W / 2) * C * 2, 0x00020000);
  auto rs_o = __builtin_amdgcn_make_buffer_rsrc((void*)out, 0, H * W * C * 2, 0x00020000);

  u32x4 cf[4][3], up[4][3];
  {
    const int gx = x0 - 1 + p;
    const bool xin = (gx >= 0) && (gx < W);
#pragma unroll
    for (int r = 0; r < 4; ++r) {
      const int gy = y0 - 1 + wave * 4 + r;
      const bool in = xin && (gy >= 0) && (gy < H);
      const unsigned oc = in ? (unsigned)((gy * W + gx) * 192 + q * 16) : 0x80000000u;
      const unsigned op = in ? (unsigned)(((gy >> 1) * (W / 2) + (gx >> 1)) * 192 + q * 16) : 0x80000000u;
#pragma unroll
      for (int ks = 0; ks < 3; ++ks) {
        cf[r][ks] = __builtin_amdgcn_raw_buffer_load_b128(rs_c, oc + ks * 64, 0, 0);
        up[r][ks] = __builtin_amdgcn_raw_buffer_load_b128(rs_p, op + ks * 64, 0, 0);
      }
    }
  }
  {
    for (int i = wave; i < 18; i += 4)
      __builtin_amdgcn_global_load_lds((const char*)w_lat + i * 1024 + lane * 16,
                                       (LDS_AS void*)(smem + W_OFF + i * 1024), 16, 0, 0);
  }
  auto stage_tap = [&](int tap, int slot) {
    for (int i = wave; i < 18; i += 4) {
      const int g = i * 64 + lane;
      const int row = g / 12, gr = g - row * 12;
      const int sgr = gr ^ (((row >> 3) & 1) << 1);
      __builtin_amdgcn_global_load_lds((const char*)w_out + tap * TAP_BYTES + row * 192 + sgr * 16,
                                       (LDS_AS void*)(smem + W_OFF + slot * TAP_BYTES + i * 1024), 16, 0, 0);
    }
  };
  for (int t = 0; t < 4; ++t) stage_tap(t, t + 1);
  __syncthreads();

  {
    bf16x8 wl[3][6];
    const int qq = (lane & 15) >> 2, pp = lane & 3;
#pragma unroll
    for (int ks = 0; ks < 3; ++ks)
#pragma unroll
      for (int t = 0; t < 6; ++t) {
        const int pair = t >> 1, h = t & 1;
        const char LDS_AS* b = smem + W_OFF + (32 * ks + 8 * q + qq) * 192 + (32 * pair + 8 * pp + 4 * h) * 2;
        wl[ks][t] = tr_frag(b, b + 4 * 192);
      }
#pragma unroll
    for (int r = 0; r < 4; ++r) {
      f32x4 acc[6];
#pragma unroll
      for (int t = 0; t < 6; ++t) {
        const int pair = t >> 1, h = t & 1;
        const unsigned u0 = up[r][pair][2 * h], u1 = up[r][pair][2 * h + 1];
        acc[t][0] = __builtin_bit_cast(float, u0 << 16);
        acc[t][1] = __builtin_bit_cast(float, u0 & 0xffff0000u);
        acc[t][2] = __builtin_bit_cast(float, u1 << 16);
        acc[t][3] = __builtin_bit_cast(float, u1 & 0xffff0000u);
      }
#pragma unroll
      for (int ks = 0; ks < 3; ++ks)
#pragma unroll
        for (int t = 0; t < 6; ++t)
          acc[t] = __builtin_amdgcn_mfma_f32_16x16x32_bf16(wl[ks][t], __builtin_bit_cast(bf16x8, cf[r][ks]), acc[t], 0, 0, 0);
#pragma unroll
      for (int pair = 0; pair < 3; ++pair) {
        u32x4 v;
        v[0] = pack_bf16(acc[2 * pair][0], acc[2 * pair][1]);
        v[1] = pack_bf16(acc[2 * pair][2], acc[2 * pair][3]);
        v[2] = pack_bf16(acc[2 * pair + 1][0], acc[2 * pair + 1][1]);
        v[3] = pack_bf16(acc[2 * pair + 1][2], acc[2 * pair + 1][3]);
        *(LDS_AS u32x4*)(smem + (4 * pair + q) * PLANE + ((wave * 4 + r) * 16 + p) * 16) = v;
      }
    }
  }

  const int nh = wave & 1, mh = wave >> 1;
  bf16x8 wo[9][3][3];
  {
    const int qq = (lane & 15) >> 2, pp = lane & 3;
    auto load_tap = [&](int tap, int slot) {
#pragma unroll
      for (int ks = 0; ks < 3; ++ks)
#pragma unroll
        for (int t = 0; t < 3; ++t) {
          const int colb = (48 * nh + 16 * t + 4 * pp) * 2;
          const int gr = colb >> 4;
          const int sgr = gr ^ ((q & 1) << 1);
          const char LDS_AS* b = smem + W_OFF + slot * TAP_BYTES + (32 * ks + 8 * q + qq) * 192 + sgr * 16 + (colb & 15);
          wo[tap][ks][t] = tr_frag(b, b + 4 * 192);
        }
    };
#pragma unroll
    for (int t = 0; t < 4; ++t) load_tap(t, t + 1);
    __syncthreads();
#pragma unroll
    for (int t = 4; t < 9; ++t) stage_tap(t, t - 4);
    __syncthreads();
#pragma unroll
    for (int t = 4; t < 9; ++t) load_tap(t, t - 4);
  }

  for (int it = 0; it < 7; ++it) {
    const int o = mh * 7 + it;
    f32x4 acc[3] = {{0, 0, 0, 0}, {0, 0, 0, 0}, {0, 0, 0, 0}};
    const char LDS_AS* mb = smem + q * PLANE + (o * 16 + p) * 16;
#pragma unroll
    for (int tap = 0; tap < 9; ++tap) {
      const int dy = tap / 3, dx = tap % 3;
#pragma unroll
      for (int ks = 0; ks < 3; ++ks) {
        bf16x8 b = *(const LDS_AS bf16x8*)(mb + ks * 4 * PLANE + (dy * 16 + dx) * 16);
#pragma unroll
        for (int t = 0; t < 3; ++t)
          acc[t] = __builtin_amdgcn_mfma_f32_16x16x32_bf16(wo[tap][ks][t], b, acc[t], 0, 0, 0);
      }
    }
    const unsigned oo = (p < TS) ? (unsigned)(((y0 + o) * W + x0 + p) * 192 + nh * 96 + q * 8) : 0x80000000u;
#pragma unroll
    for (int t = 0; t < 3; ++t) {
      u32x2 v;
      v[0] = pack_bf16(acc[t][0], acc[t][1]);
      v[1] = pack_bf16(acc[t][2], acc[t][3]);
      __builtin_amdgcn_raw_buffer_store_b64(v, rs_o, oo + t * 32, 0, 1);
    }
  }
}

extern "C" void kernel_launch(void* const* d_in, const int* in_sizes, int n_in,
                              void* d_out, int out_size, void* d_ws, size_t ws_size,
                              hipStream_t stream) {
  const __hip_bfloat16* c_fine = (const __hip_bfloat16*)d_in[0];
  const __hip_bfloat16* p_coarse = (const __hip_bfloat16*)d_in[1];
  const __hip_bfloat16* w_lat = (const __hip_bfloat16*)d_in[2];
  const __hip_bfloat16* w_out = (const __hip_bfloat16*)d_in[3];
  __hip_bfloat16* out = (__hip_bfloat16*)d_out;
  (void)hipFuncSetAttribute((const void*)fpn_fused, hipFuncAttributeMaxDynamicSharedMemorySize, LDS_BYTES);
  fpn_fused<<<256, 256, LDS_BYTES, stream>>>(c_fine, p_coarse, w_lat, w_out, out);
}
```

```cpp
#define AMP_CYC 0ull
#define AMP_RT skew
#define USE_PRE
#include <hip/hip_runtime.h>
#include <hip/hip_bf16.h>
#include <stdint.h>

typedef __attribute__((ext_vector_type(8))) short bf16x8;
typedef __attribute__((ext_vector_type(4))) short s16x4;
typedef __attribute__((ext_vector_type(4))) float f32x4;
typedef __attribute__((ext_vector_type(4))) unsigned int u32x4;
typedef __attribute__((ext_vector_type(2))) unsigned int u32x2;
typedef __attribute__((ext_vector_type(2))) __bf16 bf16x2_t;
typedef __attribute__((ext_vector_type(2))) float f32x2;

#define STAMPS 1
#define NSTAMP 10
#define NWAVES 4
#define LDS_AS __attribute__((address_space(3)))

constexpr int H = 224, W = 224, C = 96;
constexpr int TS = 14;
constexpr int PLANE = 4096;
constexpr int M_BYTES = 12 * PLANE + 64;
constexpr int W_OFF = 12 * PLANE + 256;
constexpr int TAP_BYTES = 96 * 96 * 2;
constexpr int LDS_BYTES = W_OFF + 5 * TAP_BYTES;

__device__ __forceinline__ unsigned pack_bf16(float a, float b) {
  f32x2 v = {a, b};
  bf16x2_t r = __builtin_convertvector(v, bf16x2_t);
  return __builtin_bit_cast(unsigned, r);
}

__device__ __forceinline__ bf16x8 tr_frag(const char LDS_AS* p0, const char LDS_AS* p1) {
  s16x4 a = __builtin_amdgcn_ds_read_tr16_b64_v4i16((LDS_AS s16x4*)p0);
  s16x4 b = __builtin_amdgcn_ds_read_tr16_b64_v4i16((LDS_AS s16x4*)p1);
  bf16x8 r;
  r[0] = a[0]; r[1] = a[1]; r[2] = a[2]; r[3] = a[3];
  r[4] = b[0]; r[5] = b[1]; r[6] = b[2]; r[7] = b[3];
  return r;
}

__global__ __launch_bounds__(256, 1) void fpn_fused(const __hip_bfloat16* __restrict__ c_fine,
                                                    const __hip_bfloat16* __restrict__ p_coarse,
                                                    const __hip_bfloat16* __restrict__ w_lat,
                                                    const __hip_bfloat16* __restrict__ w_out,
                                                    __hip_bfloat16* __restrict__ out, unsigned long long* __restrict__ stamps) {
  unsigned long long ts[NSTAMP], rt[NSTAMP];
#define STAMP(i) do { __builtin_amdgcn_sched_barrier(0); asm volatile("s_memtime %0\n\ts_memrealtime %1\n\ts_waitcnt lgkmcnt(0)" : "=s"(ts[i]), "=s"(rt[i]) :: "memory"); __builtin_amdgcn_sched_barrier(0);} while(0)
  asm volatile("s_memrealtime %0\n\ts_waitcnt lgkmcnt(0)" : "=s"(ts[0]) :: "memory");
  STAMP(1);
  if ((threadIdx.x & 63) == 0) atomicMin(&stamps[16], rt[1]);

  extern __shared__ __attribute__((aligned(16))) char smem_raw[];
  char LDS_AS* smem = (char LDS_AS*)smem_raw;
  const int tid = threadIdx.x;
  const int lane = tid & 63;
  const int wave = __builtin_amdgcn_readfirstlane(tid >> 6);
  const int p = lane & 15, q = lane >> 4;
  const int bid = blockIdx.x;
  const int xcd = bid & 7, idx = bid >> 3;
  const int ty = (xcd >> 1) * 4 + (idx >> 3);
  const int tx = (xcd & 1) * 8 + (idx & 7);
  const int y0 = ty * TS, x0 = tx * TS;

  auto rs_c = __builtin_amdgcn_make_buffer_rsrc((void*)c_fine, 0, H * W * C * 2, 0x00020000);
  auto rs_p = __builtin_amdgcn_make_buffer_rsrc((void*)p_coarse, 0, (H / 2) * (W / 2) * C * 2, 0x00020000);
  auto rs_o = __builtin_amdgcn_make_buffer_rsrc((void*)out, 0, H * W * C * 2, 0x00020000);

  u32x4 cf[4][3], up[4][3];
  {
    const int gx = x0 - 1 + p;
    const bool xin = (gx >= 0) && (gx < W);
#pragma unroll
    for (int r = 0; r < 4; ++r) {
      const int gy = y0 - 1 + wave * 4 + r;
      const bool in = xin && (gy >= 0) && (gy < H);
      const unsigned oc = in ? (unsigned)((gy * W + gx) * 192 + q * 16) : 0x80000000u;
      const unsigned op = in ? (unsigned)(((gy >> 1) * (W / 2) + (gx >> 1)) * 192 + q * 16) : 0x80000000u;
#pragma unroll
      for (int ks = 0; ks < 3; ++ks) {
        cf[r][ks] = __builtin_amdgcn_raw_buffer_load_b128(rs_c, oc + ks * 64, 0, 0);
        up[r][ks] = __builtin_amdgcn_raw_buffer_load_b128(rs_p, op + ks * 64, 0, 0);
      }
    }
  }
  {
    for (int i = wave; i < 18; i += 4)
      __builtin_amdgcn_global_load_lds((const char*)w_lat + i * 1024 + lane * 16,
                                       (LDS_AS void*)(smem + W_OFF + i * 1024), 16, 0, 0);
  }
  auto stage_tap = [&](int tap, int slot) {
    for (int i = wave; i < 18; i += 4) {
      const int g = i * 64 + lane;
      const int row = g / 12, gr = g - row * 12;
      const int sgr = gr ^ (((row >> 3) & 1) << 1);
      __builtin_amdgcn_global_load_lds((const char*)w_out + tap * TAP_BYTES + row * 192 + sgr * 16,
                                       (LDS_AS void*)(smem + W_OFF + slot * TAP_BYTES + i * 1024), 16, 0, 0);
    }
  };
  for (int t = 0; t < 4; ++t) stage_tap(t, t + 1);
  STAMP(2);
  __syncthreads();
  STAMP(3);

  {
    bf16x8 wl[3][6];
    const int qq = (lane & 15) >> 2, pp = lane & 3;
#pragma unroll
    for (int ks = 0; ks < 3; ++ks)
#pragma unroll
      for (int t = 0; t < 6; ++t) {
        const int pair = t >> 1, h = t & 1;
        const char LDS_AS* b = smem + W_OFF + (32 * ks + 8 * q + qq) * 192 + (32 * pair + 8 * pp + 4 * h) * 2;
        wl[ks][t] = tr_frag(b, b + 4 * 192);
      }
#pragma unroll
    for (int r = 0; r < 4; ++r) {
      f32x4 acc[6];
#pragma unroll
      for (int t = 0; t < 6; ++t) {
        const int pair = t >> 1, h = t & 1;
        const unsigned u0 = up[r][pair][2 * h], u1 = up[r][pair][2 * h + 1];
        acc[t][0] = __builtin_bit_cast(float, u0 << 16);
        acc[t][1] = __builtin_bit_cast(float, u0 & 0xffff0000u);
        acc[t][2] = __builtin_bit_cast(float, u1 << 16);
        acc[t][3] = __builtin_bit_cast(float, u1 & 0xffff0000u);
      }
#pragma unroll
      for (int ks = 0; ks < 3; ++ks)
#pragma unroll
        for (int t = 0; t < 6; ++t)
          acc[t] = __builtin_amdgcn_mfma_f32_16x16x32_bf16(wl[ks][t], __builtin_bit_cast(bf16x8, cf[r][ks]), acc[t], 0, 0, 0);
#pragma unroll
      for (int pair = 0; pair < 3; ++pair) {
        u32x4 v;
        v[0] = pack_bf16(acc[2 * pair][0], acc[2 * pair][1]);
        v[1] = pack_bf16(acc[2 * pair][2], acc[2 * pair][3]);
        v[2] = pack_bf16(acc[2 * pair + 1][0], acc[2 * pair + 1][1]);
        v[3] = pack_bf16(acc[2 * pair + 1][2], acc[2 * pair + 1][3]);
        *(LDS_AS u32x4*)(smem + (4 * pair + q) * PLANE + ((wave * 4 + r) * 16 + p) * 16) = v;
      }
    }
  }

  STAMP(4);
  const int nh = wave & 1, mh = wave >> 1;
  bf16x8 wo[9][3][3];
  {
    const int qq = (lane & 15) >> 2, pp = lane & 3;
    auto load_tap = [&](int tap, int slot) {
#pragma unroll
      for (int ks = 0; ks < 3; ++ks)
#pragma unroll
        for (int t = 0; t < 3; ++t) {
          const int colb = (48 * nh + 16 * t + 4 * pp) * 2;
          const int gr = colb >> 4;
          const int sgr = gr ^ ((q & 1) << 1);
          const char LDS_AS* b = smem + W_OFF + slot * TAP_BYTES + (32 * ks + 8 * q + qq) * 192 + sgr * 16 + (colb & 15);
          wo[tap][ks][t] = tr_frag(b, b + 4 * 192);
        }
    };
#pragma unroll
    for (int t = 0; t < 4; ++t) load_tap(t, t + 1);
    __syncthreads();
    STAMP(5);
#pragma unroll
    for (int t = 4; t < 9; ++t) stage_tap(t, t - 4);
    __syncthreads();
    STAMP(6);
#pragma unroll
    for (int t = 4; t < 9; ++t) load_tap(t, t - 4);
  }

  STAMP(7);
  for (int it = 0; it < 7; ++it) {
    const int o = mh * 7 + it;
    f32x4 acc[3] = {{0, 0, 0, 0}, {0, 0, 0, 0}, {0, 0, 0, 0}};
    const char LDS_AS* mb = smem + q * PLANE + (o * 16 + p) * 16;
#pragma unroll
    for (int tap = 0; tap < 9; ++tap) {
      const int dy = tap / 3, dx = tap % 3;
#pragma unroll
      for (int ks = 0; ks < 3; ++ks) {
        bf16x8 b = *(const LDS_AS bf16x8*)(mb + ks * 4 * PLANE + (dy * 16 + dx) * 16);
#pragma unroll
        for (int t = 0; t < 3; ++t)
          acc[t] = __builtin_amdgcn_mfma_f32_16x16x32_bf16(wo[tap][ks][t], b, acc[t], 0, 0, 0);
      }
    }
    const unsigned oo = (p < TS) ? (unsigned)(((y0 + o) * W + x0 + p) * 192 + nh * 96 + q * 8) : 0x80000000u;
#pragma unroll
    for (int t = 0; t < 3; ++t) {
      u32x2 v;
      v[0] = pack_bf16(acc[t][0], acc[t][1]);
      v[1] = pack_bf16(acc[t][2], acc[t][3]);
      __builtin_amdgcn_raw_buffer_store_b64(v, rs_o, oo + t * 32, 0, 0);
    }
  }
  STAMP(8);
  asm volatile("s_waitcnt vmcnt(0)" ::: "memory");
  STAMP(9);
  {
    unsigned long long tA = __hip_atomic_load(&stamps[0], __ATOMIC_RELAXED, __HIP_MEMORY_SCOPE_AGENT);
    unsigned long long tmin = __hip_atomic_load(&stamps[16], __ATOMIC_RELAXED, __HIP_MEMORY_SCOPE_AGENT);
    tA = ((unsigned long long)__builtin_amdgcn_readfirstlane((unsigned)(tA >> 32)) << 32) | (unsigned)__builtin_amdgcn_readfirstlane((unsigned)tA);
    tmin = ((unsigned long long)__builtin_amdgcn_readfirstlane((unsigned)(tmin >> 32)) << 32) | (unsigned)__builtin_amdgcn_readfirstlane((unsigned)tmin);
    unsigned long long gapA = tmin - tA; if (gapA > 5000ull) gapA = 5000ull;
    unsigned long long skew = rt[1] - tmin; if (skew > 5000ull) skew = 5000ull;
    unsigned long long span = rt[9] - tmin; if (span > 5000ull) span = 5000ull;
    unsigned long long tend = ts[9] + (AMP_CYC), now;
    do { asm volatile("s_sleep 2\n\ts_memtime %0\n\ts_waitcnt lgkmcnt(0)" : "=s"(now) :: "memory"); } while (now < tend);
    tend = rt[9] + (AMP_RT);
    do { asm volatile("s_sleep 2\n\ts_memrealtime %0\n\ts_waitcnt lgkmcnt(0)" : "=s"(now) :: "memory"); } while (now < tend);
  }
}

__global__ void pre_stamp(unsigned long long* ws) {
  unsigned long long t;
  asm volatile("s_memrealtime %0\n\ts_waitcnt lgkmcnt(0)" : "=s"(t) :: "memory");
  if (threadIdx.x == 0) { ws[0] = t; ws[16] = ~0ull; }
}
extern "C" void kernel_launch(void* const* d_in, const int* in_sizes, int n_in,
                              void* d_out, int out_size, void* d_ws, size_t ws_size,
                              hipStream_t stream) {
  const __hip_bfloat16* c_fine = (const __hip_bfloat16*)d_in[0];
  const __hip_bfloat16* p_coarse = (const __hip_bfloat16*)d_in[1];
  const __hip_bfloat16* w_lat = (const __hip_bfloat16*)d_in[2];
  const __hip_bfloat16* w_out = (const __hip_bfloat16*)d_in[3];
  __hip_bfloat16* out = (__hip_bfloat16*)d_out;
  (void)hipFuncSetAttribute((const void*)fpn_fused, hipFuncAttributeMaxDynamicSharedMemorySize, LDS_BYTES);
  pre_stamp<<<1, 64, 0, stream>>>((unsigned long long*)d_ws);
  fpn_fused<<<256, 256, LDS_BYTES, stream>>>(c_fine, p_coarse, w_lat, w_out, out, (unsigned long long*)d_ws);
}
```

```cpp
#include <hip/hip_runtime.h>
#include <hip/hip_bf16.h>
#include <stdint.h>

typedef __attribute__((ext_vector_type(8))) short bf16x8;
typedef __attribute__((ext_vector_type(4))) short s16x4;
typedef __attribute__((ext_vector_type(4))) float f32x4;
typedef __attribute__((ext_vector_type(4))) unsigned int u32x4;
typedef __attribute__((ext_vector_type(2))) unsigned int u32x2;
typedef __attribute__((ext_vector_type(2))) __bf16 bf16x2_t;
typedef __attribute__((ext_vector_type(2))) float f32x2;
#define LDS_AS __attribute__((address_space(3)))

#define T1 3
#define ST_AUX 0
#define B2STEP 4

constexpr int H = 224, W = 224, C = 96, TS = 14;
constexpr int T2 = 7 - T1;
constexpr int NB1 = (2 * T1 + 2 + 3) / 4;
constexpr int NB2 = 4 - NB1;
constexpr int ROWB = 3072;
constexpr int CO_OFF = 49408;
constexpr int W_OFF = 65792;
constexpr int SLAB = 18432;
constexpr int LDS_BYTES = W_OFF + 5 * SLAB;
constexpr unsigned OOB = 0x40000000u;

#define STAMP(i)
#define SB() __builtin_amdgcn_sched_barrier(0)

template <int N> __device__ __forceinline__ void waitv() { asm volatile("s_waitcnt vmcnt(%0)" :: "i"(N) : "memory"); }
__device__ __forceinline__ void bar() { asm volatile("s_waitcnt lgkmcnt(0)\n\ts_barrier" ::: "memory"); }

__device__ __forceinline__ u32x4 mk_srd(const void* p, unsigned bytes) {
  unsigned long long a = (unsigned long long)p;
  u32x4 r; r[0] = (unsigned)a; r[1] = (unsigned)(a >> 32) & 0xffffu; r[2] = bytes; r[3] = 0x00020000u; return r;
}
template <int OFF>
__device__ __forceinline__ void dma(unsigned m0v, unsigned voff, u32x4 srd, unsigned soff) {
  asm volatile("s_mov_b32 m0, %0\n\ts_nop 0\n\tbuffer_load_dwordx4 %1, %2, %3 offen offset:%4 lds" :: "s"(m0v), "v"(voff), "s"(srd), "s"(soff), "i"(OFF) : "memory");
}
__device__ __forceinline__ void dma_half(unsigned m0v, unsigned voff, u32x4 srd, unsigned soff) {
  asm volatile("s_mov_b32 m0, %0\n\ts_mov_b64 exec, 0xffffffff\n\tbuffer_load_dwordx4 %1, %2, %3 offen lds\n\ts_mov_b64 exec, -1" :: "s"(m0v), "v"(voff), "s"(srd), "s"(soff) : "memory");
}
__device__ __forceinline__ unsigned pack_bf16(float a, float b) {
  f32x2 v = {a, b};
  bf16x2_t r = __builtin_convertvector(v, bf16x2_t);
  return __builtin_bit_cast(unsigned, r);
}
__device__ __forceinline__ bf16x8 tr_frag(const char LDS_AS* p0) {
  s16x4 a = __builtin_amdgcn_ds_read_tr16_b64_v4i16((LDS_AS s16x4*)p0);
  s16x4 b = __builtin_amdgcn_ds_read_tr16_b64_v4i16((LDS_AS s16x4*)(p0 + 768));
  return __builtin_shufflevector(a, b, 0, 1, 2, 3, 4, 5, 6, 7);
}
#define MFMA(a, b, c) __builtin_amdgcn_mfma_f32_16x16x32_bf16(a, b, c, 0, 0, 0)

__global__ __launch_bounds__(256, 1) void fpn_fused(const __hip_bfloat16* __restrict__ c_fine,
                                                    const __hip_bfloat16* __restrict__ p_coarse,
                                                    const __hip_bfloat16* __restrict__ w_lat,
                                                    const __hip_bfloat16* __restrict__ w_out,
                                                    __hip_bfloat16* __restrict__ out
) {
  extern __shared__ __attribute__((aligned(16))) char smem_raw[];
  char LDS_AS* smem = (char LDS_AS*)smem_raw;
  STAMP(1);
  const int tid = threadIdx.x, lane = tid & 63;
  const int wave = __builtin_amdgcn_readfirstlane(tid >> 6);
  const int p = lane & 15, q = lane >> 4;
  const int bid = blockIdx.x;
  const int xcd = bid & 7, idx = bid >> 3;
  const int ty = (xcd >> 1) * 4 + (idx >> 3), tx = (xcd & 1) * 8 + (idx & 7);
  const int y0 = ty * TS, x0 = tx * TS;
  const u32x4 sc = mk_srd(c_fine, H * W * C * 2), sp = mk_srd(p_coarse, H * W * C / 2);
  const u32x4 sl = mk_srd(w_lat, SLAB), so = mk_srd(w_out, 9 * SLAB);
  const unsigned lb = (unsigned)(size_t)smem_raw;

  unsigned tv;
  {
    unsigned off = idx * 5184 + (wave * 11 + (lane & 15)) * 128;
    if (lane >= 11) off = OOB;
    asm volatile("buffer_load_dword %0, %1, %2, 0 offen" : "=v"(tv) : "v"(off), "s"(so) : "memory");
  }
  {
    const unsigned v = wave * 4608 + lane * 16, m = lb + W_OFF + wave * 4608;
    dma<0>(m, v, sl, 0); dma<1024>(m, v, sl, 0); dma<2048>(m, v, sl, 0); dma<3072>(m, v, sl, 0);
    dma_half(m + 4096, v + 4096, sl, 0);
  }
  unsigned vc0 = (x0 - 1) * 192 + lane * 16, vc1 = vc0, vc2 = vc0;
  if (tx == 0 && lane < 12) vc0 = OOB;
  if (tx == 15 && lane >= 52) vc2 = OOB;
  auto row_dma = [&](int i) {
    const int r = 4 * i + wave;
    const int gy = y0 - 1 + r;
    const unsigned so_ = (gy >= 0 && gy < H) ? gy * W * 192 : OOB;
    const unsigned m = lb + r * ROWB;
    dma<0>(m, vc0, sc, so_); dma<1024>(m, vc1, sc, so_); dma<2048>(m, vc2, sc, so_);
  };
#pragma unroll
  for (int i = 0; i < 4; ++i) {
    const int g = (i * 4 + wave) * 64 + lane;
    const int row = g / 108, rem = g - row * 108, px = rem / 12;
    const int cy = ty * 7 - 1 + row, cx = tx * 7 - 1 + px;
    unsigned v = (cy * 112 + (tx * 7 - 1)) * 192 + rem * 16;
    if (g >= 972 || cy < 0 || cy >= 112 || cx < 0 || cx >= 112) v = OOB;
    dma<0>(lb + CO_OFF + (i * 4 + wave) * 1024, v, sp, 0);
  }
#pragma unroll
  for (int i = 0; i < NB1; ++i) row_dma(i);
  unsigned vw[5];
#pragma unroll
  for (int i = 0; i < 5; ++i) {
    const int g = wave * 288 + i * 64 + lane;
    const int rho = g / 12, gr = g - rho * 12;
    const int sg = gr ^ (((rho >> 3) & 1) << 1);
    vw[i] = (rho >> 5) * 3 * SLAB + (rho & 31) * 192 + sg * 16;
  }
  auto slab_dma = [&](int s) {
    const int dx = s / 3, ks = s % 3;
    const unsigned so_ = dx * SLAB + ks * 6144;
    const unsigned m = lb + W_OFF + ((s + 1) % 5) * SLAB + wave * 4608;
    dma<0>(m, vw[0], so, so_); dma<0>(m + 1024, vw[1], so, so_); dma<0>(m + 2048, vw[2], so, so_); dma<0>(m + 3072, vw[3], so, so_);
    dma_half(m + 4096, vw[4], so, so_);
  };
  STAMP(2);
  waitv<3 * NB1>();
  bar();
  STAMP(3);
  slab_dma(0);
  bf16x8 wl[3][6];
  {
    const int qq = (lane & 15) >> 2, pp = lane & 3;
    const char LDS_AS* b = smem + W_OFF + (8 * q + qq) * 192 + 16 * pp;
#pragma unroll
    for (int ks = 0; ks < 3; ++ks)
#pragma unroll
      for (int t = 0; t < 6; ++t) wl[ks][t] = tr_frag(b + ks * 6144 + (t >> 1) * 64 + (t & 1) * 8);
  }
  slab_dma(1);
  auto lateral = [&](int i) {
    const int r = 4 * i + wave;
    const char LDS_AS* rb = smem + r * ROWB + p * 192 + q * 16;
    const char LDS_AS* cb = smem + CO_OFF + (((r + 1) >> 1) * 9 + ((p + 1) >> 1)) * 192 + q * 16;
    bf16x8 cf[3]; u32x4 up[3];
#pragma unroll
    for (int ks = 0; ks < 3; ++ks) { up[ks] = *(const LDS_AS u32x4*)(cb + ks * 64); cf[ks] = *(const LDS_AS bf16x8*)(rb + ks * 64); }
    f32x4 acc[6];
#pragma unroll
    for (int t = 0; t < 6; ++t) {
      const unsigned u0 = up[t >> 1][2 * (t & 1)], u1 = up[t >> 1][2 * (t & 1) + 1];
      acc[t][0] = __builtin_bit_cast(float, u0 << 16);
      acc[t][1] = __builtin_bit_cast(float, u0 & 0xffff0000u);
      acc[t][2] = __builtin_bit_cast(float, u1 << 16);
      acc[t][3] = __builtin_bit_cast(float, u1 & 0xffff0000u);
    }
#pragma unroll
    for (int ks = 0; ks < 3; ++ks)
#pragma unroll
      for (int t = 0; t < 6; ++t) acc[t] = MFMA(wl[ks][t], cf[ks], acc[t]);
    char LDS_AS* mw = smem + r * ROWB + q * 256 + p * 16;
#pragma unroll
    for (int pair = 0; pair < 3; ++pair) {
      u32x4 v;
      v[0] = pack_bf16(acc[2 * pair][0], acc[2 * pair][1]);
      v[1] = pack_bf16(acc[2 * pair][2], acc[2 * pair][3]);
      v[2] = pack_bf16(acc[2 * pair + 1][0], acc[2 * pair + 1][1]);
      v[3] = pack_bf16(acc[2 * pair + 1][2], acc[2 * pair + 1][3]);
      *(LDS_AS u32x4*)(mw + pair * 1024) = v;
    }
  };
  waitv<10>();
  STAMP(4);
#pragma unroll
  for (int i = 0; i < NB2; ++i) row_dma(NB1 + i);
  slab_dma(2);
#pragma unroll
  for (int i = 0; i < NB1; ++i) lateral(i);
  slab_dma(3);
  STAMP(5);
  waitv<15 + 3 * NB2>();
  bar();
  STAMP(6);
  slab_dma(4);

  const int nh = wave & 1, mh = wave >> 1;
  bf16x8 wo[9][3][3];
  const char LDS_AS* wb[3];
  {
    const int qq = (lane & 15) >> 2, pp = lane & 3;
#pragma unroll
    for (int t = 0; t < 3; ++t) {
      const int gr = (6 * nh + 2 * t + (pp >> 1)) ^ ((q & 1) << 1);
      wb[t] = smem + W_OFF + (8 * q + qq) * 192 + gr * 16 + (pp & 1) * 8;
    }
  }
  const char LDS_AS* mb1 = smem + (mh * T1) * ROWB + q * 256 + p * 16;
  bf16x8 mf[2][T1 + 2];
  auto read_slab = [&](int s) {
    const int dx = s / 3, ks = s % 3;
#pragma unroll
    for (int r = 0; r < T1 + 2; ++r) mf[s & 1][r] = *(const LDS_AS bf16x8*)(mb1 + r * ROWB + ks * 1024 + dx * 16);
#pragma unroll
    for (int dy = 0; dy < 3; ++dy)
#pragma unroll
      for (int t = 0; t < 3; ++t) wo[s][dy][t] = tr_frag(wb[t] + ((s + 1) % 5) * SLAB + dy * 6144);
  };
  read_slab(0);
  f32x4 acc[4][3];
#pragma unroll
  for (int o = 0; o < 4; ++o)
#pragma unroll
    for (int t = 0; t < 3; ++t) acc[o][t] = f32x4{0, 0, 0, 0};
  auto rs_o = __builtin_amdgcn_make_buffer_rsrc((void*)out, 0, H * W * C * 2, 0x00020000);
  const unsigned oo = (p < TS) ? (unsigned)((y0 * W + x0 + p) * 192 + nh * 96 + q * 8) : OOB;
  auto store_row = [&](const f32x4* a, int o) {
#pragma unroll
    for (int t = 0; t < 3; ++t) {
      u32x2 v;
      v[0] = pack_bf16(a[t][0], a[t][1]);
      v[1] = pack_bf16(a[t][2], a[t][3]);
      __builtin_amdgcn_raw_buffer_store_b64(v, rs_o, oo + t * 32, o * W * 192, ST_AUX);
    }
  };

  f32x4 acc1[T1][3];
#pragma unroll
  for (int o = 0; o < T1; ++o)
#pragma unroll
    for (int t = 0; t < 3; ++t) acc1[o][t] = f32x4{0, 0, 0, 0};
#pragma unroll
  for (int s = 0; s < 9; ++s) {
    if (s < 8) {
      if (s == 0) waitv<15 + 3 * NB2>();
      else if (s <= 4) waitv<15>();
      else if (s == 5) waitv<10>();
      else if (s == 6) waitv<5>();
      else waitv<0>();
      bar();
      if (s + 5 <= 8) slab_dma(s + 5);
      read_slab(s + 1);
    }
#pragma unroll
    for (int r = 0; r < T1 + 2; ++r)
#pragma unroll
      for (int dy = 0; dy < 3; ++dy) {
        const int o = r - dy;
        if (o >= 0 && o < T1) {
#pragma unroll
          for (int t = 0; t < 3; ++t) acc1[o][t] = MFMA(wo[s][dy][t], mf[s & 1][r], acc1[o][t]);
        }
      }
    STAMP(7 + s);
    if (s == B2STEP) {
#pragma unroll
      for (int i = 0; i < NB2; ++i) lateral(NB1 + i);
      STAMP(16);
    }
  }
#pragma unroll
  for (int o = 0; o < T1; ++o) store_row(acc1[o], mh * T1 + o);
  STAMP(17);

  if constexpr (T2 > 0) {
    const char LDS_AS* mb2 = smem + (2 * T1 + mh * T2) * ROWB + q * 256 + p * 16;
    bf16x8 mq[2][9];
#pragma unroll
    for (int s = 0; s < 9; ++s) mq[0][s] = *(const LDS_AS bf16x8*)(mb2 + (s % 3) * 1024 + (s / 3) * 16);
#pragma unroll
    for (int r = 0; r < T2 + 2; ++r) {
#pragma unroll
      for (int s = 0; s < 9; ++s) {
        if (r + 1 < T2 + 2) mq[(r + 1) & 1][s] = *(const LDS_AS bf16x8*)(mb2 + (r + 1) * ROWB + (s % 3) * 1024 + (s / 3) * 16);
#pragma unroll
        for (int dy = 2; dy >= 0; --dy) {
          const int o = r - dy;
          if (o >= 0 && o < T2) {
#pragma unroll
            for (int t = 0; t < 3; ++t) acc[o][t] = MFMA(wo[s][dy][t], mq[r & 1][s], acc[o][t]);
          }
        }
        SB();
      }
      if (r >= 2) store_row(acc[r - 2], 2 * T1 + mh * T2 + r - 2);
    }
  }
  STAMP(18);
  asm volatile("" :: "v"(tv));
}

extern "C" void kernel_launch(void* const* d_in, const int* in_sizes, int n_in,
                              void* d_out, int out_size, void* d_ws, size_t ws_size,
                              hipStream_t stream) {
  const __hip_bfloat16* c_fine = (const __hip_bfloat16*)d_in[0];
  const __hip_bfloat16* p_coarse = (const __hip_bfloat16*)d_in[1];
  const __hip_bfloat16* w_lat = (const __hip_bfloat16*)d_in[2];
  const __hip_bfloat16* w_out = (const __hip_bfloat16*)d_in[3];
  __hip_bfloat16* out = (__hip_bfloat16*)d_out;
  (void)hipFuncSetAttribute((const void*)fpn_fused, hipFuncAttributeMaxDynamicSharedMemorySize, LDS_BYTES);
  fpn_fused<<<256, 256, LDS_BYTES, stream>>>(c_fine, p_coarse, w_lat, w_out, out
  );
}
```

```cpp
#include <hip/hip_runtime.h>
#include <hip/hip_bf16.h>
#include <stdint.h>


__global__ __launch_bounds__(768, 3) void fpn_fused(const __hip_bfloat16* __restrict__ c_fine,
                                                    const __hip_bfloat16* __restrict__ p_coarse,
                                                    const __hip_bfloat16* __restrict__ w_lat,
                                                    const __hip_bfloat16* __restrict__ w_out,
                                                    __hip_bfloat16* __restrict__ out) {
  __shared__ __attribute__((aligned(16))) char smem[163840];
  register unsigned tid asm("v0") = threadIdx.x;
  asm volatile(
    "s_load_dwordx2 s[16:17], %1, 16\n\t"
    "s_load_dwordx2 s[20:21], %1, 24\n\t"
    "s_load_dwordx2 s[12:13], %1, 8\n\t"
    "s_load_dwordx2 s[8:9], %1, 0\n\t"
    "s_load_dwordx2 s[24:25], %1, 32\n\t"
    "v_readfirstlane_b32 s30, v0\n\t"
    "v_and_b32 v1, 63, v0\n\t"
    "s_lshr_b32 s30, s30, 6\n\t"
    "v_lshlrev_b32 v2, 4, v1\n\t"
    "s_and_b32 s31, s30, 3\n\t"
    "s_and_b32 s35, %2, 6\n\t"
    "s_lshr_b32 s36, %2, 6\n\t"
    "s_lshl1_add_u32 s6, s35, s36\n\t"
    "s_bfe_u32 s35, %2, 0x30003\n\t"
    "s_and_b32 s36, %2, 1\n\t"
    "s_lshl3_add_u32 s33, s36, s35\n\t"
    "s_lshl2_add_u32 s7, s30, 161280\n\t"
    "s_cmp_eq_u32 s35, s30\n\t"
    "s_cbranch_scc0 L_notouch\n\t"
    "s_getpc_b64 s[46:47]\n\t"
    "L_pc:\n\t"
    "v_lshlrev_b32 v126, 7, v0\n\t"
    "v_min_u32 v126, L_end-L_pc, v126\n\t"
    "global_load_dword v127, v126, s[46:47]\n\t"
    "L_notouch:\n\t"
    "s_cmp_ge_u32 s30, 4\n\t"
    "s_cbranch_scc1 L_lat\n\t"
    "s_mov_b32 s49, 1073741824\n\t"
    "v_mov_b32 v36, s49\n\t"
    "v_add_u32 v16, s7, v2\n\t"
    "s_mul_i32 s40, s31, 3072\n\t"
    "ds_write_b32 v16, v36\n\t"
    "s_mov_b32 s18, 18432\n\t"
    "s_mov_b32 s19, 0x20000\n\t"
    "s_and_b32 s36, s31, 1\n\t"
    "s_mul_i32 s47, s36, 3072\n\t"
    "s_add_u32 s48, s47, 12288\n\t"
    "s_mov_b32 s22, 165888\n\t"
    "s_mov_b32 s23, 0x20000\n\t"
    "s_lshr_b32 s35, %2, 3\n\t"
    "s_mul_i32 s44, s35, 5184\n\t"
    "s_mul_i32 s5, s31, 1408\n\t"
    "s_add_u32 s44, s44, s5\n\t"
    "v_cmp_gt_u32 vcc, 11, v1\n\t"
    "v_lshl_add_u32 v85, v1, 7, s44\n\t"
    "s_nop 0\n\t"
    "v_cndmask_b32 v85, v36, v85, vcc\n\t"
    "s_mov_b32 s14, 2408448\n\t"
    "s_mov_b32 s15, 0x20000\n\t"
    "s_mul_i32 s44, s33, 1344\n\t"
    "s_sub_u32 s44, s44, 192\n\t"
    "s_add_u32 s45, s44, 1728\n\t"
    "s_min_u32 s45, s45, 21504\n\t"
    "s_and_b32 s36, s31, 1\n\t"
    "s_lshl_b32 s36, s36, 10\n\t"
    "s_add_u32 s44, s44, s36\n\t"
    "v_add_u32 v86, s44, v2\n\t"
    "v_cmp_gt_u32 vcc, s45, v86\n\t"
    "s_lshr_b32 s36, s31, 1\n\t"
    "s_mul_i32 s39, s6, 7\n\t"
    "s_add_u32 s39, s39, s36\n\t"
    "s_mul_i32 s39, s39, 21504\n\t"
    "v_cndmask_b32 v86, v36, v86, vcc\n\t"
    "s_lshl_b32 s42, s31, 10\n\t"
    "s_waitcnt lgkmcnt(0)\n\t"
    "s_setprio 3\n\t"
    "s_add_u32 m0, s40, 142464\n\t"
    "s_nop 0\n\t"
    "buffer_load_dwordx4 v2, s[16:19], s40 offen offset:0 lds\n\t"
    "buffer_load_dwordx4 v2, s[16:19], s40 offen offset:1024 lds\n\t"
    "buffer_load_dwordx4 v2, s[16:19], s40 offen offset:2048 lds\n\t"
    "s_bitcmp1_b32 s31, 1\n\t"
    "s_cbranch_scc0 L_skip0\n\t"
    "s_add_u32 m0, s48, 142464\n\t"
    "s_nop 0\n\t"
    "buffer_load_dwordx4 v2, s[16:19], s48 offen offset:0 lds\n\t"
    "buffer_load_dwordx4 v2, s[16:19], s48 offen offset:1024 lds\n\t"
    "buffer_load_dwordx4 v2, s[16:19], s48 offen offset:2048 lds\n\t"
    "L_skip0:\n\t"
    "s_add_u32 m0, s7, 48\n\t"
    "s_mov_b64 exec, 1\n\t"
    "buffer_load_dword v36, s[16:19], 0 offen lds\n\t"
    "s_mov_b64 exec, -1\n\t"
    "buffer_load_dword v85, v85, s[20:23], 0 offen\n\t"
    "s_sub_u32 s44, s39, 21504\n\t"
    "s_cselect_b32 s44, s49, s44\n\t"
    "s_add_u32 m0, s42, 123648\n\t"
    "s_nop 0\n\t"
    "buffer_load_dwordx4 v86, s[12:15], s44 offen lds\n\t"
    "s_add_u32 s44, s39, 21504\n\t"
    "s_add_u32 m0, s42, 127744\n\t"
    "s_nop 0\n\t"
    "buffer_load_dwordx4 v86, s[12:15], s44 offen lds\n\t"
    "s_add_u32 s44, s39, 64512\n\t"
    "s_add_u32 m0, s42, 131840\n\t"
    "s_nop 0\n\t"
    "buffer_load_dwordx4 v86, s[12:15], s44 offen lds\n\t"
    "s_add_u32 s44, s39, 107520\n\t"
    "s_add_u32 m0, s42, 135936\n\t"
    "s_nop 0\n\t"
    "buffer_load_dwordx4 v86, s[12:15], s44 offen lds\n\t"
    "s_add_u32 s44, s39, 150528\n\t"
    "s_add_u32 m0, s42, 140032\n\t"
    "s_cmp_lt_u32 s31, 2\n\t"
    "s_cbranch_scc0 L_noc8\n\t"
    "buffer_load_dwordx4 v86, s[12:15], s44 offen lds\n\t"
    "L_noc8:\n\t"
    "s_add_u32 m0, s7, 64\n\t"
    "s_mov_b64 exec, 1\n\t"
    "buffer_load_dword v36, s[12:15], 0 offen lds\n\t"
    "s_mov_b64 exec, -1\n\t"
    "s_mov_b32 s10, 9633792\n\t"
    "s_mov_b32 s11, 0x20000\n\t"
    "s_mul_i32 s44, s33, 2688\n\t"
    "s_sub_u32 s44, s44, 192\n\t"
    "v_lshrrev_b32 v16, 2, v1\n\t"
    "v_mul_u32_u24 v16, 43, v16\n\t"
    "v_add_u32 v17, 1935, v16\n\t"
    "v_bfe_u32 v17, v17, 9, 2\n\t"
    "v_lshlrev_b32 v17, 4, v17\n\t"
    "v_xad_u32 v88, v17, v2, s44\n\t"
    "s_add_u32 s44, s44, 1024\n\t"
    "v_cmp_gt_u32 vcc, 43008, v88\n\t"
    "s_and_b32 s4, s31, 1\n\t"
    "s_mul_i32 s4, s4, 7\n\t"
    "s_lshr_b32 s5, s31, 1\n\t"
    "s_add_u32 s5, s5, s4\n\t"
    "v_cndmask_b32 v88, v36, v88, vcc\n\t"
    "v_add_u32 v17, 2623, v16\n\t"
    "v_bfe_u32 v17, v17, 9, 2\n\t"
    "v_lshlrev_b32 v17, 4, v17\n\t"
    "v_xad_u32 v89, v17, v2, s44\n\t"
    "s_add_u32 s44, s44, 1024\n\t"
    "v_add_u32 v17, 3311, v16\n\t"
    "v_bfe_u32 v17, v17, 9, 2\n\t"
    "v_lshlrev_b32 v17, 4, v17\n\t"
    "v_xad_u32 v90, v17, v2, s44\n\t"
    "v_cmp_gt_u32 vcc, 43008, v90\n\t"
    "s_mul_i32 s4, s6, 14\n\t"
    "s_add_u32 s4, s4, s5\n\t"
    "s_mul_i32 s4, s4, 43008\n\t"
    "v_cndmask_b32 v90, v36, v90, vcc\n\t"
    "s_mul_i32 s41, s5, 5376\n\t"
    "s_and_b32 s5, s5, 1\n\t"
    "s_lshl_b32 s5, s5, 6\n\t"
    "s_add_u32 s41, s41, s5\n\t"
    "s_sub_u32 s44, s4, 43008\n\t"
    "s_cselect_b32 s44, s49, s44\n\t"
    "s_add_u32 m0, s41, 0\n\t"
    "s_nop 0\n\t"
    "buffer_load_dwordx4 v88, s[8:11], s44 offen nt lds\n\t"
    "s_add_u32 m0, s41, 1024\n\t"
    "s_nop 0\n\t"
    "buffer_load_dwordx4 v89, s[8:11], s44 offen nt lds\n\t"
    "s_add_u32 m0, s41, 2048\n\t"
    "s_nop 0\n\t"
    "buffer_load_dwordx4 v90, s[8:11], s44 offen nt lds\n\t"
    "s_add_u32 m0, s7, 256\n\t"
    "s_mov_b64 exec, 1\n\t"
    "buffer_load_dword v36, s[8:11], 0 offen lds\n\t"
    "s_mov_b64 exec, -1\n\t"
    "s_barrier\n\t"
    "s_lshr_b32 s46, s31, 1\n\t"
    "s_lshl_b32 s44, s46, 7\n\t"
    "s_mul_i32 s46, s46, 18432\n\t"
    "s_add_u32 s46, s46, s47\n\t"
    "v_and_b32 v83, 32, v1\n\t"
    "v_xor_b32 v84, 32, v2\n\t"
    "v_xor_b32 v83, v83, v2\n\t"
    "s_add_u32 s47, s47, 36864\n\t"
    "s_add_u32 s40, s40, s44\n\t"
    "s_add_u32 s48, s48, 256\n\t"
    "s_add_u32 m0, s40, 86016\n\t"
    "s_nop 0\n\t"
    "buffer_load_dwordx4 v2, s[20:23], s46 offen offset:0 lds\n\t"
    "buffer_load_dwordx4 v83, s[20:23], s46 offen offset:1024 lds\n\t"
    "buffer_load_dwordx4 v84, s[20:23], s46 offen offset:2048 lds\n\t"
    "s_bitcmp0_b32 s31, 1\n\t"
    "s_cbranch_scc0 L_skip1\n\t"
    "s_add_u32 m0, s48, 86016\n\t"
    "s_nop 0\n\t"
    "buffer_load_dwordx4 v2, s[20:23], s47 offen offset:0 lds\n\t"
    "buffer_load_dwordx4 v83, s[20:23], s47 offen offset:1024 lds\n\t"
    "buffer_load_dwordx4 v84, s[20:23], s47 offen offset:2048 lds\n\t"
    "L_skip1:\n\t"
    "s_add_u32 m0, s7, 80\n\t"
    "s_mov_b64 exec, 1\n\t"
    "buffer_load_dword v36, s[20:23], 0 offen lds\n\t"
    "s_mov_b64 exec, -1\n\t"
    "s_add_u32 s44, s4, 43008\n\t"
    "s_add_u32 m0, s41, 10752\n\t"
    "s_nop 0\n\t"
    "buffer_load_dwordx4 v88, s[8:11], s44 offen nt lds\n\t"
    "s_add_u32 m0, s41, 11776\n\t"
    "s_nop 0\n\t"
    "buffer_load_dwordx4 v89, s[8:11], s44 offen nt lds\n\t"
    "s_add_u32 m0, s41, 12800\n\t"
    "s_nop 0\n\t"
    "buffer_load_dwordx4 v90, s[8:11], s44 offen nt lds\n\t"
    "s_add_u32 m0, s7, 272\n\t"
    "s_mov_b64 exec, 1\n\t"
    "buffer_load_dword v36, s[8:11], 0 offen lds\n\t"
    "s_mov_b64 exec, -1\n\t"
    "s_add_u32 s44, s4, 129024\n\t"
    "s_add_u32 m0, s41, 21504\n\t"
    "s_nop 0\n\t"
    "buffer_load_dwordx4 v88, s[8:11], s44 offen nt lds\n\t"
    "s_add_u32 m0, s41, 22528\n\t"
    "s_nop 0\n\t"
    "buffer_load_dwordx4 v89, s[8:11], s44 offen nt lds\n\t"
    "s_add_u32 m0, s41, 23552\n\t"
    "s_nop 0\n\t"
    "buffer_load_dwordx4 v90, s[8:11], s44 offen nt lds\n\t"
    "s_add_u32 m0, s7, 288\n\t"
    "s_mov_b64 exec, 1\n\t"
    "buffer_load_dword v36, s[8:11], 0 offen lds\n\t"
    "s_mov_b64 exec, -1\n\t"
    "s_cmp_eq_u32 s31, 2\n\t"
    "s_cselect_b32 s44, 344064, 0\n\t"
    "s_cselect_b32 s5, 43008, 0\n\t"
    "s_add_u32 s44, s44, s4\n\t"
    "s_add_u32 s44, s44, 215040\n\t"
    "s_add_u32 s41, s41, s5\n\t"
    "s_add_u32 m0, s41, 32256\n\t"
    "s_nop 0\n\t"
    "buffer_load_dwordx4 v88, s[8:11], s44 offen nt lds\n\t"
    "s_add_u32 m0, s41, 33280\n\t"
    "s_nop 0\n\t"
    "buffer_load_dwordx4 v89, s[8:11], s44 offen nt lds\n\t"
    "s_add_u32 m0, s41, 34304\n\t"
    "s_nop 0\n\t"
    "buffer_load_dwordx4 v90, s[8:11], s44 offen nt lds\n\t"
    "s_add_u32 m0, s7, 304\n\t"
    "s_mov_b64 exec, 1\n\t"
    "buffer_load_dword v36, s[8:11], 0 offen lds\n\t"
    "s_mov_b64 exec, -1\n\t"
    "s_and_b32 s44, s31, 2\n\t"
    "s_add_u32 s37, s31, s44\n\t"
    "s_mov_b32 s43, 0x100000\n\t"
    "s_mov_b32 s44, 161280\n\t"
    "v_lshl_add_u32 v7, v1, 2, s44\n\t"
    "v_mov_b32 v9, s7\n\t"
    "v_lshrrev_b32 v16, 4, v1\n\t"
    "v_and_b32 v17, 7, v1\n\t"
    "v_bfe_u32 v18, v1, 3, 1\n\t"
    "v_mov_b32 v19, 196608\n\t"
    "v_mul_u32_u24 v20, 112, v16\n\t"
    "s_mov_b32 s44, 37632\n\t"
    "v_lshl_add_u32 v20, v17, 4, v20\n\t"
    "s_mov_b32 vcc_lo, 0x7f7f7f7f\n\t"
    "s_mov_b32 vcc_hi, vcc_lo\n\t"
    "v_mad_u32_u24 v3, v18, s44, v20\n\t"
    "v_bfe_u32 v21, v1, 2, 2\n\t"
    "v_lshl_add_u32 v21, v16, 3, v21\n\t"
    "v_mul_u32_u24 v21, 192, v21\n\t"
    "v_and_b32 v22, 3, v1\n\t"
    "s_cmp_ge_u32 s37, 3\n\t"
    "s_cselect_b32 s34, 1, 0\n\t"
    "s_cselect_b32 s35, 3, 0\n\t"
    "s_sub_u32 s35, s37, s35\n\t"
    "s_mul_i32 s44, s35, 896\n\t"
    "s_add_u32 s44, s44, 86016\n\t"
    "v_add_u32 v11, s44, v3\n\t"
    "s_mul_i32 s44, s34, 1344\n\t"
    "s_add_u32 s44, s44, 2688\n\t"
    "v_add_u32 v3, s44, v3\n\t"
    "v_cndmask_b32 v3, v19, v3, vcc\n\t"
    "v_cndmask_b32 v11, v19, v11, vcc\n\t"
    "v_and_b32 v5, 1, v16\n\t"
    "s_mul_i32 s44, s34, 12544\n\t"
    "s_lshl_b32 s36, s35, 6\n\t"
    "s_add_u32 s44, s44, s36\n\t"
    "s_add_u32 s44, s44, 86016\n\t"
    "v_mul_u32_u24 v4, 384, v17\n\t"
    "v_lshl_add_u32 v4, v5, 5, v4\n\t"
    "v_lshl_add_u32 v5, v5, 5, v21\n\t"
    "v_lshl_add_u32 v5, v22, 3, v5\n\t"
    "v_add_u32 v5, s44, v5\n\t"
    "v_xor_b32 v6, 32, v5\n\t"
    "s_lshl_b32 s44, s35, 1\n\t"
    "s_lshl_b32 s38, 48, s44\n\t"
    "v_and_b32 v21, 2, v16\n\t"
    "s_mov_b32 s44, 301056\n\t"
    "v_lshl_add_u32 v4, v21, 3, v4\n\t"
    "v_mad_u32_u24 v4, v18, s44, v4\n\t"
    "v_mov_b32 v21, 1073741824\n\t"
    "s_mul_i32 s44, s6, 602112\n\t"
    "s_mul_i32 s39, s33, 2688\n\t"
    "s_add_u32 s39, s39, s44\n\t"
    "v_cndmask_b32 v4, v21, v4, vcc\n\t"
    "s_lshl_b32 s44, s37, 6\n\t"
    "s_add_u32 s39, s39, s44\n\t"
    "s_mov_b32 s26, 9633792\n\t"
    "s_mov_b32 s27, 0x20000\n\t"
    "s_mul_i32 s44, s34, 2688\n\t"
    "v_add_u32 v1, s44, v11\n\t"
    "v_and_b32 v21, 16, v2\n\t"
    "s_lshl_b32 s44, s34, 15\n\t"
    "s_or_b32 s44, s44, 0x3f80\n\t"
    "v_lshlrev_b32 v22, v21, s44\n\t"
    "s_mov_b32 vcc_lo, 0x00300003\n\t"
    "s_mov_b32 vcc_hi, 0x30000300\n\t"
    "v_and_b32 v21, 0x7fff7fff, v22\n\t"
    "v_cndmask_b32 v14, 0, v22, vcc\n\t"
    "v_cndmask_b32 v12, 0, v21, vcc\n\t"
    "s_lshl_b64 vcc, vcc, 2\n\t"
    "v_cndmask_b32 v15, 0, v22, vcc\n\t"
    "v_cndmask_b32 v13, 0, v21, vcc\n\t"
    "ds_read_b32 v8, v7\n\t"
    "ds_read_b64_tr_b16 v[96:97], v5 offset:0\n\t"
    "ds_read_b64_tr_b16 v[98:99], v5 offset:768\n\t"
    "ds_read_b64_tr_b16 v[100:101], v6 offset:0\n\t"
    "ds_read_b64_tr_b16 v[102:103], v6 offset:768\n\t"
    "s_waitcnt lgkmcnt(4)\n\t"
    "v_cmp_lt_u32 vcc, 0, v8\n\t"
    "s_and_b32 s44, vcc_lo, 0xf00000\n\t"
    "s_cbranch_scc1 L_poll10\n\t"
    "L_polld10:\n\t"
    "v_mov_b32 v10, 8\n\t"
    "ds_write_b32 v9, v10\n\t"
    "s_waitcnt vmcnt(4)\n\t"
    "L_poll11:\n\t"
    "ds_read_b32 v8, v7\n\t"
    "s_waitcnt lgkmcnt(0)\n\t"
    "v_cmp_lt_u32 vcc, 9, v8\n\t"
    "s_and_b32 s44, vcc_lo, 0xff0\n\t"
    "s_cbranch_scc0 L_polld11\n\t"
    "s_sleep 1\n\t"
    "s_sub_u32 s43, s43, 1\n\t"
    "s_cbranch_scc1 L_end\n\t"
    "s_branch L_poll11\n\t"
    "L_polld11:\n\t"
    "s_add_u32 s45, s46, 55296\n\t"
    "s_add_u32 m0, s40, 104832\n\t"
    "s_nop 0\n\t"
    "buffer_load_dwordx4 v2, s[20:23], s45 offen offset:0 lds\n\t"
    "s_nop 15\n\t"
    "s_nop 15\n\t"
    "s_nop 7\n\t"
    "buffer_load_dwordx4 v83, s[20:23], s45 offen offset:1024 lds\n\t"
    "s_nop 15\n\t"
    "s_nop 15\n\t"
    "s_nop 7\n\t"
    "buffer_load_dwordx4 v84, s[20:23], s45 offen offset:2048 lds\n\t"
    "s_nop 15\n\t"
    "s_nop 15\n\t"
    "s_nop 4\n\t"
    "s_bitcmp1_b32 s31, 1\n\t"
    "s_cbranch_scc0 L_skip2\n\t"
    "s_add_u32 s45, s47, 55296\n\t"
    "s_add_u32 m0, s48, 104832\n\t"
    "s_nop 0\n\t"
    "buffer_load_dwordx4 v2, s[20:23], s45 offen offset:0 lds\n\t"
    "s_nop 15\n\t"
    "s_nop 15\n\t"
    "s_nop 7\n\t"
    "buffer_load_dwordx4 v83, s[20:23], s45 offen offset:1024 lds\n\t"
    "s_nop 15\n\t"
    "s_nop 15\n\t"
    "s_nop 7\n\t"
    "buffer_load_dwordx4 v84, s[20:23], s45 offen offset:2048 lds\n\t"
    "s_nop 15\n\t"
    "s_nop 15\n\t"
    "s_nop 4\n\t"
    "L_skip2:\n\t"
    "s_add_u32 m0, s7, 96\n\t"
    "s_mov_b64 exec, 1\n\t"
    "buffer_load_dword v36, s[20:23], 0 offen lds\n\t"
    "s_mov_b64 exec, -1\n\t"
    "s_add_u32 s45, s46, 110592\n\t"
    "s_add_u32 m0, s40, 123648\n\t"
    "s_nop 0\n\t"
    "buffer_load_dwordx4 v2, s[20:23], s45 offen offset:0 lds\n\t"
    "s_nop 15\n\t"
    "s_nop 15\n\t"
    "s_nop 7\n\t"
    "buffer_load_dwordx4 v83, s[20:23], s45 offen offset:1024 lds\n\t"
    "s_nop 15\n\t"
    "s_nop 15\n\t"
    "s_nop 7\n\t"
    "buffer_load_dwordx4 v84, s[20:23], s45 offen offset:2048 lds\n\t"
    "s_nop 15\n\t"
    "s_nop 15\n\t"
    "s_nop 4\n\t"
    "s_bitcmp0_b32 s31, 1\n\t"
    "s_cbranch_scc0 L_skip3\n\t"
    "s_add_u32 s45, s47, 110592\n\t"
    "s_add_u32 m0, s48, 123648\n\t"
    "s_nop 0\n\t"
    "buffer_load_dwordx4 v2, s[20:23], s45 offen offset:0 lds\n\t"
    "s_nop 15\n\t"
    "s_nop 15\n\t"
    "s_nop 7\n\t"
    "buffer_load_dwordx4 v83, s[20:23], s45 offen offset:1024 lds\n\t"
    "s_nop 15\n\t"
    "s_nop 15\n\t"
    "s_nop 7\n\t"
    "buffer_load_dwordx4 v84, s[20:23], s45 offen offset:2048 lds\n\t"
    "s_nop 15\n\t"
    "s_nop 15\n\t"
    "s_nop 4\n\t"
    "L_skip3:\n\t"
    "s_add_u32 m0, s7, 112\n\t"
    "s_mov_b64 exec, 1\n\t"
    "buffer_load_dword v36, s[20:23], 0 offen lds\n\t"
    "s_mov_b64 exec, -1\n\t"
    "s_add_u32 s45, s46, 6144\n\t"
    "s_add_u32 m0, s40, 142464\n\t"
    "s_nop 0\n\t"
    "buffer_load_dwordx4 v2, s[20:23], s45 offen offset:0 lds\n\t"
    "s_nop 15\n\t"
    "s_nop 13\n\t"
    "buffer_load_dwordx4 v83, s[20:23], s45 offen offset:1024 lds\n\t"
    "s_nop 15\n\t"
    "s_nop 13\n\t"
    "buffer_load_dwordx4 v84, s[20:23], s45 offen offset:2048 lds\n\t"
    "ds_read_b32 v8, v7\n\t"
    "s_nop 15\n\t"
    "s_nop 10\n\t"
    "s_bitcmp1_b32 s31, 1\n\t"
    "s_cbranch_scc0 L_skip4\n\t"
    "s_add_u32 s45, s47, 6144\n\t"
    "s_add_u32 m0, s48, 142464\n\t"
    "s_nop 0\n\t"
    "buffer_load_dwordx4 v2, s[20:23], s45 offen offset:0 lds\n\t"
    "s_nop 15\n\t"
    "s_nop 13\n\t"
    "buffer_load_dwordx4 v83, s[20:23], s45 offen offset:1024 lds\n\t"
    "s_nop 15\n\t"
    "s_nop 13\n\t"
    "buffer_load_dwordx4 v84, s[20:23], s45 offen offset:2048 lds\n\t"
    "ds_read_b32 v8, v7\n\t"
    "s_nop 15\n\t"
    "s_nop 10\n\t"
    "L_skip4:\n\t"
    "s_add_u32 m0, s7, 128\n\t"
    "s_mov_b64 exec, 1\n\t"
    "buffer_load_dword v36, s[20:23], 0 offen lds\n\t"
    "s_mov_b64 exec, -1\n\t"
    "s_waitcnt lgkmcnt(0)\n\t"
    "v_cmp_lt_u32 vcc, 8, v8\n\t"
    "s_and_b32 s44, vcc_lo, 0xfff\n\t"
    "s_cbranch_scc1 L_slow12\n\t"
    "L_cont12:\n\t"
    "s_add_u32 s45, s46, 61440\n\t"
    "s_add_u32 m0, s40, 86016\n\t"
    "s_nop 0\n\t"
    "buffer_load_dwordx4 v2, s[20:23], s45 offen offset:0 lds\n\t"
    "v_cmp_lt_u32 vcc, 0, v8\n\t"
    "s_and_b32 s44, vcc_lo, 0xf000000\n\t"
    "s_cbranch_scc1 L_slow13\n\t"
    "L_cont13:\n\t"
    "ds_read_b64_tr_b16 v[104:105], v5 offset:18816\n\t"
    "ds_read_b64_tr_b16 v[106:107], v5 offset:19584\n\t"
    "ds_read_b64_tr_b16 v[108:109], v6 offset:18816\n\t"
    "ds_read_b64_tr_b16 v[110:111], v6 offset:19584\n\t"
    "v_mov_b32 v10, 7\n\t"
    "ds_write_b32 v9, v10\n\t"
    "buffer_load_dwordx4 v83, s[20:23], s45 offen offset:1024 lds\n\t"
    "s_nop 15\n\t"
    "buffer_load_dwordx4 v84, s[20:23], s45 offen offset:2048 lds\n\t"
    "ds_read_b32 v8, v7\n\t"
    "s_nop 12\n\t"
    "s_bitcmp0_b32 s31, 1\n\t"
    "s_cbranch_scc0 L_skip5\n\t"
    "s_add_u32 s45, s47, 61440\n\t"
    "s_add_u32 m0, s48, 86016\n\t"
    "s_nop 0\n\t"
    "buffer_load_dwordx4 v2, s[20:23], s45 offen offset:0 lds\n\t"
    "s_nop 15\n\t"
    "buffer_load_dwordx4 v83, s[20:23], s45 offen offset:1024 lds\n\t"
    "s_nop 15\n\t"
    "buffer_load_dwordx4 v84, s[20:23], s45 offen offset:2048 lds\n\t"
    "ds_read_b32 v8, v7\n\t"
    "s_nop 12\n\t"
    "L_skip5:\n\t"
    "s_add_u32 m0, s7, 144\n\t"
    "s_mov_b64 exec, 1\n\t"
    "buffer_load_dword v36, s[20:23], 0 offen lds\n\t"
    "s_mov_b64 exec, -1\n\t"
    "s_waitcnt lgkmcnt(0)\n\t"
    "v_cmp_lt_u32 vcc, 7, v8\n\t"
    "s_and_b32 s44, vcc_lo, 0xfff\n\t"
    "s_cbranch_scc1 L_slow14\n\t"
    "L_cont14:\n\t"
    "s_add_u32 s45, s46, 116736\n\t"
    "s_add_u32 m0, s40, 104832\n\t"
    "s_nop 0\n\t"
    "buffer_load_dwordx4 v2, s[20:23], s45 offen offset:0 lds\n\t"
    "v_cmp_lt_u32 vcc, 0, v8\n\t"
    "s_and_b32 s44, vcc_lo, 0xf0000000\n\t"
    "s_cbranch_scc1 L_slow15\n\t"
    "L_cont15:\n\t"
    "ds_read_b64_tr_b16 v[112:113], v5 offset:37632\n\t"
    "ds_read_b64_tr_b16 v[114:115], v5 offset:38400\n\t"
    "ds_read_b64_tr_b16 v[116:117], v6 offset:37632\n\t"
    "ds_read_b64_tr_b16 v[118:119], v6 offset:38400\n\t"
    "v_mov_b32 v10, 6\n\t"
    "ds_write_b32 v9, v10\n\t"
    "buffer_load_dwordx4 v83, s[20:23], s45 offen offset:1024 lds\n\t"
    "s_nop 13\n\t"
    "buffer_load_dwordx4 v84, s[20:23], s45 offen offset:2048 lds\n\t"
    "ds_read_b32 v8, v7\n\t"
    "s_nop 10\n\t"
    "s_bitcmp1_b32 s31, 1\n\t"
    "s_cbranch_scc0 L_skip6\n\t"
    "s_add_u32 s45, s47, 116736\n\t"
    "s_add_u32 m0, s48, 104832\n\t"
    "s_nop 0\n\t"
    "buffer_load_dwordx4 v2, s[20:23], s45 offen offset:0 lds\n\t"
    "s_nop 13\n\t"
    "buffer_load_dwordx4 v83, s[20:23], s45 offen offset:1024 lds\n\t"
    "s_nop 13\n\t"
    "buffer_load_dwordx4 v84, s[20:23], s45 offen offset:2048 lds\n\t"
    "ds_read_b32 v8, v7\n\t"
    "s_nop 10\n\t"
    "L_skip6:\n\t"
    "s_add_u32 m0, s7, 160\n\t"
    "s_mov_b64 exec, 1\n\t"
    "buffer_load_dword v36, s[20:23], 0 offen lds\n\t"
    "s_mov_b64 exec, -1\n\t"
    "s_waitcnt lgkmcnt(0)\n\t"
    "v_cmp_lt_u32 vcc, 6, v8\n\t"
    "s_and_b32 s44, vcc_lo, 0xfff\n\t"
    "s_cbranch_scc1 L_slow16\n\t"
    "L_cont16:\n\t"
    "s_add_u32 s45, s46, 12288\n\t"
    "s_add_u32 m0, s40, 123648\n\t"
    "s_nop 0\n\t"
    "buffer_load_dwordx4 v2, s[20:23], s45 offen offset:0 lds\n\t"
    "v_cmp_lt_u32 vcc, 0, v8\n\t"
    "s_and_b32 s44, vcc_hi, 0xf\n\t"
    "s_cbranch_scc1 L_slow17\n\t"
    "L_cont17:\n\t"
    "ds_read_b64_tr_b16 v[120:121], v5 offset:56448\n\t"
    "ds_read_b64_tr_b16 v[122:123], v5 offset:57216\n\t"
    "ds_read_b64_tr_b16 v[124:125], v6 offset:56448\n\t"
    "ds_read_b64_tr_b16 v[126:127], v6 offset:57216\n\t"
    "v_mov_b32 v10, 5\n\t"
    "ds_write_b32 v9, v10\n\t"
    "buffer_load_dwordx4 v83, s[20:23], s45 offen offset:1024 lds\n\t"
    "s_nop 13\n\t"
    "buffer_load_dwordx4 v84, s[20:23], s45 offen offset:2048 lds\n\t"
    "ds_read_b32 v8, v7\n\t"
    "s_nop 10\n\t"
    "s_bitcmp0_b32 s31, 1\n\t"
    "s_cbranch_scc0 L_skip7\n\t"
    "s_add_u32 s45, s47, 12288\n\t"
    "s_add_u32 m0, s48, 123648\n\t"
    "s_nop 0\n\t"
    "buffer_load_dwordx4 v2, s[20:23], s45 offen offset:0 lds\n\t"
    "s_nop 13\n\t"
    "buffer_load_dwordx4 v83, s[20:23], s45 offen offset:1024 lds\n\t"
    "s_nop 13\n\t"
    "buffer_load_dwordx4 v84, s[20:23], s45 offen offset:2048 lds\n\t"
    "ds_read_b32 v8, v7\n\t"
    "s_nop 10\n\t"
    "L_skip7:\n\t"
    "s_add_u32 m0, s7, 176\n\t"
    "s_mov_b64 exec, 1\n\t"
    "buffer_load_dword v36, s[20:23], 0 offen lds\n\t"
    "s_mov_b64 exec, -1\n\t"
    "s_waitcnt lgkmcnt(0)\n\t"
    "v_cmp_lt_u32 vcc, 5, v8\n\t"
    "s_and_b32 s44, vcc_lo, 0xfff\n\t"
    "s_cbranch_scc1 L_slow18\n\t"
    "L_cont18:\n\t"
    "s_add_u32 s45, s46, 67584\n\t"
    "s_add_u32 m0, s40, 142464\n\t"
    "s_nop 0\n\t"
    "buffer_load_dwordx4 v2, s[20:23], s45 offen offset:0 lds\n\t"
    "v_cmp_lt_u32 vcc, 0, v8\n\t"
    "s_and_b32 s44, vcc_hi, 0xf0\n\t"
    "s_cbranch_scc1 L_slow19\n\t"
    "L_cont19:\n\t"
    "ds_read_b64_tr_b16 v[128:129], v5 offset:0\n\t"
    "ds_read_b64_tr_b16 v[130:131], v5 offset:768\n\t"
    "ds_read_b64_tr_b16 v[132:133], v6 offset:0\n\t"
    "ds_read_b64_tr_b16 v[134:135], v6 offset:768\n\t"
    "v_mov_b32 v10, 4\n\t"
    "ds_write_b32 v9, v10\n\t"
    "buffer_load_dwordx4 v83, s[20:23], s45 offen offset:1024 lds\n\t"
    "s_nop 13\n\t"
    "buffer_load_dwordx4 v84, s[20:23], s45 offen offset:2048 lds\n\t"
    "ds_read_b32 v8, v7\n\t"
    "s_nop 10\n\t"
    "s_bitcmp1_b32 s31, 1\n\t"
    "s_cbranch_scc0 L_skip8\n\t"
    "s_add_u32 s45, s47, 67584\n\t"
    "s_add_u32 m0, s48, 142464\n\t"
    "s_nop 0\n\t"
    "buffer_load_dwordx4 v2, s[20:23], s45 offen offset:0 lds\n\t"
    "s_nop 13\n\t"
    "buffer_load_dwordx4 v83, s[20:23], s45 offen offset:1024 lds\n\t"
    "s_nop 13\n\t"
    "buffer_load_dwordx4 v84, s[20:23], s45 offen offset:2048 lds\n\t"
    "ds_read_b32 v8, v7\n\t"
    "s_nop 10\n\t"
    "L_skip8:\n\t"
    "s_add_u32 m0, s7, 192\n\t"
    "s_mov_b64 exec, 1\n\t"
    "buffer_load_dword v36, s[20:23], 0 offen lds\n\t"
    "s_mov_b64 exec, -1\n\t"
    "s_waitcnt lgkmcnt(0)\n\t"
    "v_cmp_lt_u32 vcc, 4, v8\n\t"
    "s_and_b32 s44, vcc_lo, 0xfff\n\t"
    "s_cbranch_scc1 L_slow20\n\t"
    "L_cont20:\n\t"
    "s_add_u32 s45, s46, 122880\n\t"
    "s_add_u32 m0, s40, 86016\n\t"
    "s_nop 0\n\t"
    "buffer_load_dwordx4 v2, s[20:23], s45 offen offset:0 lds\n\t"
    "v_cmp_lt_u32 vcc, 0, v8\n\t"
    "s_and_b32 s44, vcc_hi, 0xf00\n\t"
    "s_cbranch_scc1 L_slow21\n\t"
    "L_cont21:\n\t"
    "ds_read_b64_tr_b16 v[136:137], v5 offset:18816\n\t"
    "ds_read_b64_tr_b16 v[138:139], v5 offset:19584\n\t"
    "ds_read_b64_tr_b16 v[140:141], v6 offset:18816\n\t"
    "ds_read_b64_tr_b16 v[142:143], v6 offset:19584\n\t"
    "buffer_load_dwordx4 v83, s[20:23], s45 offen offset:1024 lds\n\t"
    "s_nop 13\n\t"
    "buffer_load_dwordx4 v84, s[20:23], s45 offen offset:2048 lds\n\t"
    "s_nop 10\n\t"
    "s_bitcmp0_b32 s31, 1\n\t"
    "s_cbranch_scc0 L_skip9\n\t"
    "s_add_u32 s45, s47, 122880\n\t"
    "s_add_u32 m0, s48, 86016\n\t"
    "s_nop 0\n\t"
    "buffer_load_dwordx4 v2, s[20:23], s45 offen offset:0 lds\n\t"
    "s_nop 13\n\t"
    "buffer_load_dwordx4 v83, s[20:23], s45 offen offset:1024 lds\n\t"
    "s_nop 13\n\t"
    "buffer_load_dwordx4 v84, s[20:23], s45 offen offset:2048 lds\n\t"
    "s_nop 10\n\t"
    "L_skip9:\n\t"
    "s_add_u32 m0, s7, 208\n\t"
    "s_mov_b64 exec, 1\n\t"
    "buffer_load_dword v36, s[20:23], 0 offen lds\n\t"
    "s_mov_b64 exec, -1\n\t"
    "ds_read_b32 v8, v7\n\t"
    "ds_read_b64_tr_b16 v[144:145], v5 offset:37632\n\t"
    "ds_read_b64_tr_b16 v[146:147], v5 offset:38400\n\t"
    "ds_read_b64_tr_b16 v[148:149], v6 offset:37632\n\t"
    "ds_read_b64_tr_b16 v[150:151], v6 offset:38400\n\t"
    "s_waitcnt lgkmcnt(4)\n\t"
    "v_cmp_lt_u32 vcc, 0, v8\n\t"
    "s_and_b32 s44, vcc_hi, 0xf000\n\t"
    "s_cbranch_scc1 L_poll22\n\t"
    "L_polld22:\n\t"
    "ds_read_b32 v8, v7\n\t"
    "ds_read_b64_tr_b16 v[152:153], v5 offset:56448\n\t"
    "ds_read_b64_tr_b16 v[154:155], v5 offset:57216\n\t"
    "ds_read_b64_tr_b16 v[156:157], v6 offset:56448\n\t"
    "ds_read_b64_tr_b16 v[158:159], v6 offset:57216\n\t"
    "s_waitcnt lgkmcnt(4)\n\t"
    "v_cmp_lt_u32 vcc, 0, v8\n\t"
    "s_and_b32 s44, vcc_hi, 0xf0000\n\t"
    "s_cbranch_scc1 L_poll23\n\t"
    "L_polld23:\n\t"
    "v_mov_b32 v10, 3\n\t"
    "ds_write_b32 v9, v10\n\t"
    "ds_read_b32 v8, v7\n\t"
    "ds_read_b64_tr_b16 v[160:161], v5 offset:0\n\t"
    "ds_read_b64_tr_b16 v[162:163], v5 offset:768\n\t"
    "ds_read_b64_tr_b16 v[164:165], v6 offset:0\n\t"
    "ds_read_b64_tr_b16 v[166:167], v6 offset:768\n\t"
    "s_waitcnt lgkmcnt(4)\n\t"
    "v_cmp_lt_u32 vcc, 0, v8\n\t"
    "s_and_b32 s44, vcc_hi, 0xf00000\n\t"
    "s_cbranch_scc1 L_poll24\n\t"
    "L_polld24:\n\t"
    "v_cmp_lt_u32 vcc, 0, v8\n\t"
    "s_and_b32 s44, vcc_hi, 0xff000000\n\t"
    "s_cbranch_scc1 L_slow25\n\t"
    "L_cont25:\n\t"
    "ds_read_b128 v[48:51], v3 offset:0\n\t"
    "ds_read_b128 v[52:55], v3 offset:448\n\t"
    "ds_read_b128 v[64:67], v3 offset:896\n\t"
    "ds_read_b128 v[56:59], v3 offset:5376\n\t"
    "s_setprio 0\n\t"
    "s_waitcnt lgkmcnt(3)\n\t"
    "v_mfma_f32_16x16x32_bf16 v[40:43], v[96:99], v[48:51], 0\n\t"
    "v_mfma_f32_16x16x32_bf16 v[44:47], v[100:103], v[48:51], 0\n\t"
    "ds_read_b128 v[68:71], v3 offset:5824\n\t"
    "s_waitcnt lgkmcnt(3)\n\t"
    "v_mfma_f32_16x16x32_bf16 v[40:43], v[120:123], v[52:55], v[40:43]\n\t"
    "v_mfma_f32_16x16x32_bf16 v[44:47], v[124:127], v[52:55], v[44:47]\n\t"
    "s_waitcnt lgkmcnt(2)\n\t"
    "v_mfma_f32_16x16x32_bf16 v[40:43], v[144:147], v[64:67], v[40:43]\n\t"
    "v_mfma_f32_16x16x32_bf16 v[44:47], v[148:151], v[64:67], v[44:47]\n\t"
    "ds_read_b128 v[60:63], v3 offset:6272\n\t"
    "s_waitcnt lgkmcnt(2)\n\t"
    "v_mfma_f32_16x16x32_bf16 v[40:43], v[104:107], v[56:59], v[40:43]\n\t"
    "v_mfma_f32_16x16x32_bf16 v[44:47], v[108:111], v[56:59], v[44:47]\n\t"
    "v_mfma_f32_16x16x32_bf16 v[48:51], v[96:99], v[56:59], 0\n\t"
    "v_mfma_f32_16x16x32_bf16 v[52:55], v[100:103], v[56:59], 0\n\t"
    "ds_read_b128 v[72:75], v3 offset:10752\n\t"
    "s_waitcnt lgkmcnt(2)\n\t"
    "v_mfma_f32_16x16x32_bf16 v[40:43], v[128:131], v[68:71], v[40:43]\n\t"
    "v_mfma_f32_16x16x32_bf16 v[44:47], v[132:135], v[68:71], v[44:47]\n\t"
    "v_mfma_f32_16x16x32_bf16 v[48:51], v[120:123], v[68:71], v[48:51]\n\t"
    "v_mfma_f32_16x16x32_bf16 v[52:55], v[124:127], v[68:71], v[52:55]\n\t"
    "ds_read_b32 v8, v7\n\t"
    "s_waitcnt lgkmcnt(2)\n\t"
    "v_mfma_f32_16x16x32_bf16 v[40:43], v[152:155], v[60:63], v[40:43]\n\t"
    "v_mfma_f32_16x16x32_bf16 v[44:47], v[156:159], v[60:63], v[44:47]\n\t"
    "ds_read_b128 v[76:79], v3 offset:11200\n\t"
    "v_mfma_f32_16x16x32_bf16 v[48:51], v[144:147], v[60:63], v[48:51]\n\t"
    "v_mfma_f32_16x16x32_bf16 v[52:55], v[148:151], v[60:63], v[52:55]\n\t"
    "s_waitcnt lgkmcnt(2)\n\t"
    "v_mfma_f32_16x16x32_bf16 v[40:43], v[112:115], v[72:75], v[40:43]\n\t"
    "s_waitcnt lgkmcnt(1)\n\t"
    "v_cmp_lt_u32 vcc, 2, v8\n\t"
    "v_mfma_f32_16x16x32_bf16 v[44:47], v[116:119], v[72:75], v[44:47]\n\t"
    "s_and_b32 s44, vcc_lo, s38\n\t"
    "s_cbranch_scc1 L_slow26\n\t"
    "L_cont26:\n\t"
    "v_mfma_f32_16x16x32_bf16 v[48:51], v[104:107], v[72:75], v[48:51]\n\t"
    "ds_read_b128 v[64:67], v11 offset:18816\n\t"
    "v_mfma_f32_16x16x32_bf16 v[52:55], v[108:111], v[72:75], v[52:55]\n\t"
    "ds_read_b128 v[68:71], v11 offset:19264\n\t"
    "ds_read_b128 v[80:83], v3 offset:11648\n\t"
    "v_mfma_f32_16x16x32_bf16 v[56:59], v[96:99], v[72:75], 0\n\t"
    "v_mfma_f32_16x16x32_bf16 v[60:63], v[100:103], v[72:75], 0\n\t"
    "s_waitcnt lgkmcnt(3)\n\t"
    "v_mfma_f32_16x16x32_bf16 v[40:43], v[136:139], v[76:79], v[40:43]\n\t"
    "v_mfma_f32_16x16x32_bf16 v[44:47], v[140:143], v[76:79], v[44:47]\n\t"
    "v_mfma_f32_16x16x32_bf16 v[48:51], v[128:131], v[76:79], v[48:51]\n\t"
    "ds_read_b128 v[84:87], v3 offset:16128\n\t"
    "v_mfma_f32_16x16x32_bf16 v[52:55], v[132:135], v[76:79], v[52:55]\n\t"
    "s_waitcnt lgkmcnt(3)\n\t"
    "v_mfma_f32_16x16x32_bf16 v[40:43], v[12:15], v[64:67], v[40:43]\n\t"
    "s_waitcnt lgkmcnt(2)\n\t"
    "v_mfma_f32_16x16x32_bf16 v[44:47], v[12:15], v[68:71], v[44:47]\n\t"
    "ds_read_b32 v8, v7\n\t"
    "v_mfma_f32_16x16x32_bf16 v[56:59], v[120:123], v[76:79], v[56:59]\n\t"
    "v_mfma_f32_16x16x32_bf16 v[60:63], v[124:127], v[76:79], v[60:63]\n\t"
    "s_waitcnt lgkmcnt(2)\n\t"
    "v_mfma_f32_16x16x32_bf16 v[40:43], v[160:163], v[80:83], v[40:43]\n\t"
    "v_mfma_f32_16x16x32_bf16 v[44:47], v[164:167], v[80:83], v[44:47]\n\t"
    "v_mfma_f32_16x16x32_bf16 v[48:51], v[152:155], v[80:83], v[48:51]\n\t"
    "ds_read_b128 v[88:91], v3 offset:16576\n\t"
    "s_waitcnt lgkmcnt(1)\n\t"
    "v_cmp_lt_u32 vcc, 1, v8\n\t"
    "v_mfma_f32_16x16x32_bf16 v[52:55], v[156:159], v[80:83], v[52:55]\n\t"
    "s_and_b32 s44, vcc_lo, s38\n\t"
    "s_cbranch_scc1 L_slow27\n\t"
    "L_cont27:\n\t"
    "v_mfma_f32_16x16x32_bf16 v[56:59], v[144:147], v[80:83], v[56:59]\n\t"
    "ds_read_b128 v[72:75], v11 offset:21504\n\t"
    "v_mfma_f32_16x16x32_bf16 v[60:63], v[148:151], v[80:83], v[60:63]\n\t"
    "ds_read_b128 v[76:79], v11 offset:21952\n\t"
    "v_mfma_f32_16x16x32_bf16 v[48:51], v[112:115], v[84:87], v[48:51]\n\t"
    "v_cvt_pk_bf16_f32 v40, v40, v41\n\t"
    "v_mfma_f32_16x16x32_bf16 v[52:55], v[116:119], v[84:87], v[52:55]\n\t"
    "v_cvt_pk_bf16_f32 v41, v42, v43\n\t"
    "v_mfma_f32_16x16x32_bf16 v[56:59], v[104:107], v[84:87], v[56:59]\n\t"
    "v_cvt_pk_bf16_f32 v42, v44, v45\n\t"
    "v_mfma_f32_16x16x32_bf16 v[60:63], v[108:111], v[84:87], v[60:63]\n\t"
    "v_cvt_pk_bf16_f32 v43, v46, v47\n\t"
    "ds_read_b128 v[92:95], v3 offset:17024\n\t"
    "v_mfma_f32_16x16x32_bf16 v[64:67], v[96:99], v[84:87], 0\n\t"
    "v_permlane16_swap_b32 v40, v42\n\t"
    "v_mfma_f32_16x16x32_bf16 v[68:71], v[100:103], v[84:87], 0\n\t"
    "v_permlane16_swap_b32 v41, v43\n\t"
    "s_waitcnt lgkmcnt(3)\n\t"
    "v_mfma_f32_16x16x32_bf16 v[48:51], v[136:139], v[88:91], v[48:51]\n\t"
    "buffer_store_dwordx4 v[40:43], v4, s[24:27], s39 offen sc0 sc1\n\t"
    "s_add_u32 s39, s39, 43008\n\t"
    "v_mfma_f32_16x16x32_bf16 v[52:55], v[140:143], v[88:91], v[52:55]\n\t"
    "s_waitcnt lgkmcnt(1)\n\t"
    "v_mfma_f32_16x16x32_bf16 v[48:51], v[12:15], v[72:75], v[48:51]\n\t"
    "v_mfma_f32_16x16x32_bf16 v[52:55], v[12:15], v[76:79], v[52:55]\n\t"
    "ds_read_b32 v8, v7\n\t"
    "v_mfma_f32_16x16x32_bf16 v[56:59], v[128:131], v[88:91], v[56:59]\n\t"
    "v_mfma_f32_16x16x32_bf16 v[60:63], v[132:135], v[88:91], v[60:63]\n\t"
    "ds_read_b128 v[40:43], v3 offset:21504\n\t"
    "v_mfma_f32_16x16x32_bf16 v[64:67], v[120:123], v[88:91], v[64:67]\n\t"
    "v_mfma_f32_16x16x32_bf16 v[68:71], v[124:127], v[88:91], v[68:71]\n\t"
    "s_waitcnt lgkmcnt(2)\n\t"
    "v_mfma_f32_16x16x32_bf16 v[48:51], v[160:163], v[92:95], v[48:51]\n\t"
    "s_waitcnt lgkmcnt(1)\n\t"
    "v_cmp_lt_u32 vcc, 0, v8\n\t"
    "v_mfma_f32_16x16x32_bf16 v[52:55], v[164:167], v[92:95], v[52:55]\n\t"
    "s_and_b32 s44, vcc_lo, s38\n\t"
    "s_cbranch_scc1 L_slow28\n\t"
    "L_cont28:\n\t"
    "v_mfma_f32_16x16x32_bf16 v[56:59], v[152:155], v[92:95], v[56:59]\n\t"
    "ds_read_b128 v[80:83], v11 offset:24192\n\t"
    "v_mfma_f32_16x16x32_bf16 v[60:63], v[156:159], v[92:95], v[60:63]\n\t"
    "ds_read_b128 v[84:87], v11 offset:24640\n\t"
    "ds_read_b128 v[88:91], v3 offset:21952\n\t"
    "v_mfma_f32_16x16x32_bf16 v[64:67], v[144:147], v[92:95], v[64:67]\n\t"
    "v_mfma_f32_16x16x32_bf16 v[68:71], v[148:151], v[92:95], v[68:71]\n\t"
    "v_cvt_pk_bf16_f32 v48, v48, v49\n\t"
    "s_waitcnt lgkmcnt(3)\n\t"
    "v_mfma_f32_16x16x32_bf16 v[56:59], v[112:115], v[40:43], v[56:59]\n\t"
    "v_cvt_pk_bf16_f32 v49, v50, v51\n\t"
    "v_mfma_f32_16x16x32_bf16 v[60:63], v[116:119], v[40:43], v[60:63]\n\t"
    "v_cvt_pk_bf16_f32 v50, v52, v53\n\t"
    "v_mfma_f32_16x16x32_bf16 v[64:67], v[104:107], v[40:43], v[64:67]\n\t"
    "v_cvt_pk_bf16_f32 v51, v54, v55\n\t"
    "v_mfma_f32_16x16x32_bf16 v[68:71], v[108:111], v[40:43], v[68:71]\n\t"
    "v_permlane16_swap_b32 v48, v50\n\t"
    "ds_read_b128 v[44:47], v3 offset:22400\n\t"
    "v_mfma_f32_16x16x32_bf16 v[72:75], v[96:99], v[40:43], 0\n\t"
    "v_permlane16_swap_b32 v49, v51\n\t"
    "v_mfma_f32_16x16x32_bf16 v[76:79], v[100:103], v[40:43], 0\n\t"
    "buffer_store_dwordx4 v[48:51], v4, s[24:27], s39 offen sc0 sc1\n\t"
    "s_add_u32 s39, s39, 43008\n\t"
    "s_waitcnt lgkmcnt(1)\n\t"
    "v_mfma_f32_16x16x32_bf16 v[56:59], v[136:139], v[88:91], v[56:59]\n\t"
    "v_mfma_f32_16x16x32_bf16 v[56:59], v[12:15], v[80:83], v[56:59]\n\t"
    "v_mfma_f32_16x16x32_bf16 v[60:63], v[12:15], v[84:87], v[60:63]\n\t"
    "v_mfma_f32_16x16x32_bf16 v[60:63], v[140:143], v[88:91], v[60:63]\n\t"
    "v_mfma_f32_16x16x32_bf16 v[64:67], v[128:131], v[88:91], v[64:67]\n\t"
    "v_mfma_f32_16x16x32_bf16 v[68:71], v[132:135], v[88:91], v[68:71]\n\t"
    "ds_read_b128 v[48:51], v3 offset:26880\n\t"
    "v_mfma_f32_16x16x32_bf16 v[72:75], v[120:123], v[88:91], v[72:75]\n\t"
    "v_mfma_f32_16x16x32_bf16 v[76:79], v[124:127], v[88:91], v[76:79]\n\t"
    "s_waitcnt lgkmcnt(1)\n\t"
    "v_mfma_f32_16x16x32_bf16 v[56:59], v[160:163], v[44:47], v[56:59]\n\t"
    "v_mfma_f32_16x16x32_bf16 v[60:63], v[164:167], v[44:47], v[60:63]\n\t"
    "ds_read_b128 v[88:91], v11 offset:26880\n\t"
    "v_mfma_f32_16x16x32_bf16 v[64:67], v[152:155], v[44:47], v[64:67]\n\t"
    "ds_read_b128 v[92:95], v11 offset:27328\n\t"
    "v_mfma_f32_16x16x32_bf16 v[68:71], v[156:159], v[44:47], v[68:71]\n\t"
    "ds_read_b128 v[40:43], v3 offset:27328\n\t"
    "v_mfma_f32_16x16x32_bf16 v[72:75], v[144:147], v[44:47], v[72:75]\n\t"
    "v_mfma_f32_16x16x32_bf16 v[76:79], v[148:151], v[44:47], v[76:79]\n\t"
    "v_cvt_pk_bf16_f32 v56, v56, v57\n\t"
    "s_waitcnt lgkmcnt(3)\n\t"
    "v_mfma_f32_16x16x32_bf16 v[64:67], v[112:115], v[48:51], v[64:67]\n\t"
    "v_cvt_pk_bf16_f32 v57, v58, v59\n\t"
    "v_mfma_f32_16x16x32_bf16 v[68:71], v[116:119], v[48:51], v[68:71]\n\t"
    "v_cvt_pk_bf16_f32 v58, v60, v61\n\t"
    "v_mfma_f32_16x16x32_bf16 v[72:75], v[104:107], v[48:51], v[72:75]\n\t"
    "v_cvt_pk_bf16_f32 v59, v62, v63\n\t"
    "v_mfma_f32_16x16x32_bf16 v[76:79], v[108:111], v[48:51], v[76:79]\n\t"
    "v_permlane16_swap_b32 v56, v58\n\t"
    "ds_read_b128 v[44:47], v3 offset:27776\n\t"
    "v_mfma_f32_16x16x32_bf16 v[80:83], v[96:99], v[48:51], 0\n\t"
    "v_permlane16_swap_b32 v57, v59\n\t"
    "v_mfma_f32_16x16x32_bf16 v[84:87], v[100:103], v[48:51], 0\n\t"
    "buffer_store_dwordx4 v[56:59], v4, s[24:27], s39 offen sc0 sc1\n\t"
    "s_add_u32 s39, s39, 43008\n\t"
    "s_waitcnt lgkmcnt(1)\n\t"
    "v_mfma_f32_16x16x32_bf16 v[64:67], v[136:139], v[40:43], v[64:67]\n\t"
    "v_mfma_f32_16x16x32_bf16 v[64:67], v[12:15], v[88:91], v[64:67]\n\t"
    "v_mfma_f32_16x16x32_bf16 v[68:71], v[12:15], v[92:95], v[68:71]\n\t"
    "v_mfma_f32_16x16x32_bf16 v[68:71], v[140:143], v[40:43], v[68:71]\n\t"
    "v_mfma_f32_16x16x32_bf16 v[72:75], v[128:131], v[40:43], v[72:75]\n\t"
    "v_mfma_f32_16x16x32_bf16 v[76:79], v[132:135], v[40:43], v[76:79]\n\t"
    "ds_read_b128 v[48:51], v3 offset:32256\n\t"
    "v_mfma_f32_16x16x32_bf16 v[80:83], v[120:123], v[40:43], v[80:83]\n\t"
    "v_mfma_f32_16x16x32_bf16 v[84:87], v[124:127], v[40:43], v[84:87]\n\t"
    "s_waitcnt lgkmcnt(1)\n\t"
    "v_mfma_f32_16x16x32_bf16 v[64:67], v[160:163], v[44:47], v[64:67]\n\t"
    "v_mfma_f32_16x16x32_bf16 v[68:71], v[164:167], v[44:47], v[68:71]\n\t"
    "ds_read_b128 v[56:59], v11 offset:29568\n\t"
    "v_mfma_f32_16x16x32_bf16 v[72:75], v[152:155], v[44:47], v[72:75]\n\t"
    "ds_read_b128 v[60:63], v11 offset:30016\n\t"
    "v_mfma_f32_16x16x32_bf16 v[76:79], v[156:159], v[44:47], v[76:79]\n\t"
    "ds_read_b128 v[40:43], v3 offset:32704\n\t"
    "v_mfma_f32_16x16x32_bf16 v[80:83], v[144:147], v[44:47], v[80:83]\n\t"
    "v_mfma_f32_16x16x32_bf16 v[84:87], v[148:151], v[44:47], v[84:87]\n\t"
    "v_cvt_pk_bf16_f32 v64, v64, v65\n\t"
    "s_waitcnt lgkmcnt(3)\n\t"
    "v_mfma_f32_16x16x32_bf16 v[72:75], v[112:115], v[48:51], v[72:75]\n\t"
    "v_cvt_pk_bf16_f32 v65, v66, v67\n\t"
    "v_mfma_f32_16x16x32_bf16 v[76:79], v[116:119], v[48:51], v[76:79]\n\t"
    "v_cvt_pk_bf16_f32 v66, v68, v69\n\t"
    "v_mfma_f32_16x16x32_bf16 v[80:83], v[104:107], v[48:51], v[80:83]\n\t"
    "v_cvt_pk_bf16_f32 v67, v70, v71\n\t"
    "v_mfma_f32_16x16x32_bf16 v[84:87], v[108:111], v[48:51], v[84:87]\n\t"
    "v_permlane16_swap_b32 v64, v66\n\t"
    "ds_read_b128 v[44:47], v3 offset:33152\n\t"
    "v_mfma_f32_16x16x32_bf16 v[88:91], v[96:99], v[48:51], 0\n\t"
    "v_permlane16_swap_b32 v65, v67\n\t"
    "v_mfma_f32_16x16x32_bf16 v[92:95], v[100:103], v[48:51], 0\n\t"
    "buffer_store_dwordx4 v[64:67], v4, s[24:27], s39 offen sc0 sc1\n\t"
    "s_add_u32 s39, s39, 43008\n\t"
    "s_waitcnt lgkmcnt(1)\n\t"
    "v_mfma_f32_16x16x32_bf16 v[72:75], v[136:139], v[40:43], v[72:75]\n\t"
    "v_mfma_f32_16x16x32_bf16 v[72:75], v[12:15], v[56:59], v[72:75]\n\t"
    "v_mfma_f32_16x16x32_bf16 v[76:79], v[12:15], v[60:63], v[76:79]\n\t"
    "v_mfma_f32_16x16x32_bf16 v[76:79], v[140:143], v[40:43], v[76:79]\n\t"
    "ds_read_b128 v[56:59], v11 offset:32256\n\t"
    "v_mfma_f32_16x16x32_bf16 v[80:83], v[128:131], v[40:43], v[80:83]\n\t"
    "ds_read_b128 v[60:63], v11 offset:32704\n\t"
    "v_mfma_f32_16x16x32_bf16 v[84:87], v[132:135], v[40:43], v[84:87]\n\t"
    "ds_read_b128 v[64:67], v3 offset:37632\n\t"
    "v_mfma_f32_16x16x32_bf16 v[88:91], v[120:123], v[40:43], v[88:91]\n\t"
    "v_mfma_f32_16x16x32_bf16 v[92:95], v[124:127], v[40:43], v[92:95]\n\t"
    "s_waitcnt lgkmcnt(3)\n\t"
    "v_mfma_f32_16x16x32_bf16 v[72:75], v[160:163], v[44:47], v[72:75]\n\t"
    "ds_read_b128 v[40:43], v3 offset:38080\n\t"
    "v_mfma_f32_16x16x32_bf16 v[76:79], v[164:167], v[44:47], v[76:79]\n\t"
    "ds_read_b128 v[124:127], v11 offset:34944\n\t"
    "v_mfma_f32_16x16x32_bf16 v[80:83], v[152:155], v[44:47], v[80:83]\n\t"
    "ds_read_b128 v[120:123], v11 offset:35392\n\t"
    "v_mfma_f32_16x16x32_bf16 v[84:87], v[156:159], v[44:47], v[84:87]\n\t"
    "s_waitcnt lgkmcnt(5)\n\t"
    "v_mfma_f32_16x16x32_bf16 v[80:83], v[12:15], v[56:59], v[80:83]\n\t"
    "s_waitcnt lgkmcnt(4)\n\t"
    "v_mfma_f32_16x16x32_bf16 v[84:87], v[12:15], v[60:63], v[84:87]\n\t"
    "v_mfma_f32_16x16x32_bf16 v[88:91], v[144:147], v[44:47], v[88:91]\n\t"
    "ds_read_b128 v[48:51], v3 offset:38528\n\t"
    "v_mfma_f32_16x16x32_bf16 v[92:95], v[148:151], v[44:47], v[92:95]\n\t"
    "v_cvt_pk_bf16_f32 v72, v72, v73\n\t"
    "s_waitcnt lgkmcnt(4)\n\t"
    "v_mfma_f32_16x16x32_bf16 v[80:83], v[112:115], v[64:67], v[80:83]\n\t"
    "v_cvt_pk_bf16_f32 v73, v74, v75\n\t"
    "v_mfma_f32_16x16x32_bf16 v[84:87], v[116:119], v[64:67], v[84:87]\n\t"
    "v_cvt_pk_bf16_f32 v74, v76, v77\n\t"
    "v_mfma_f32_16x16x32_bf16 v[88:91], v[104:107], v[64:67], v[88:91]\n\t"
    "v_cvt_pk_bf16_f32 v75, v78, v79\n\t"
    "v_mfma_f32_16x16x32_bf16 v[92:95], v[108:111], v[64:67], v[92:95]\n\t"
    "v_permlane16_swap_b32 v72, v74\n\t"
    "ds_read_b128 v[44:47], v3 offset:43008\n\t"
    "s_waitcnt lgkmcnt(3)\n\t"
    "v_mfma_f32_16x16x32_bf16 v[80:83], v[136:139], v[40:43], v[80:83]\n\t"
    "v_permlane16_swap_b32 v73, v75\n\t"
    "v_mfma_f32_16x16x32_bf16 v[84:87], v[140:143], v[40:43], v[84:87]\n\t"
    "buffer_store_dwordx4 v[72:75], v4, s[24:27], s39 offen sc0 sc1\n\t"
    "s_add_u32 s39, s39, 43008\n\t"
    "ds_read_b128 v[64:67], v3 offset:43456\n\t"
    "v_mfma_f32_16x16x32_bf16 v[88:91], v[128:131], v[40:43], v[88:91]\n\t"
    "v_mfma_f32_16x16x32_bf16 v[88:91], v[12:15], v[124:127], v[88:91]\n\t"
    "s_waitcnt lgkmcnt(2)\n\t"
    "v_mfma_f32_16x16x32_bf16 v[92:95], v[12:15], v[120:123], v[92:95]\n\t"
    "v_mfma_f32_16x16x32_bf16 v[92:95], v[132:135], v[40:43], v[92:95]\n\t"
    "ds_read_b128 v[76:79], v3 offset:43904\n\t"
    "v_mfma_f32_16x16x32_bf16 v[80:83], v[160:163], v[48:51], v[80:83]\n\t"
    "v_mfma_f32_16x16x32_bf16 v[84:87], v[164:167], v[48:51], v[84:87]\n\t"
    "v_mfma_f32_16x16x32_bf16 v[88:91], v[152:155], v[48:51], v[88:91]\n\t"
    "v_mfma_f32_16x16x32_bf16 v[92:95], v[156:159], v[48:51], v[92:95]\n\t"
    "s_nop 4\n\t"
    "v_cvt_pk_bf16_f32 v80, v80, v81\n\t"
    "v_cvt_pk_bf16_f32 v81, v82, v83\n\t"
    "s_waitcnt lgkmcnt(2)\n\t"
    "v_mfma_f32_16x16x32_bf16 v[88:91], v[112:115], v[44:47], v[88:91]\n\t"
    "v_cvt_pk_bf16_f32 v82, v84, v85\n\t"
    "v_cvt_pk_bf16_f32 v83, v86, v87\n\t"
    "v_mfma_f32_16x16x32_bf16 v[92:95], v[116:119], v[44:47], v[92:95]\n\t"
    "v_permlane16_swap_b32 v80, v82\n\t"
    "v_permlane16_swap_b32 v81, v83\n\t"
    "s_waitcnt lgkmcnt(1)\n\t"
    "v_mfma_f32_16x16x32_bf16 v[88:91], v[136:139], v[64:67], v[88:91]\n\t"
    "buffer_store_dwordx4 v[80:83], v4, s[24:27], s39 offen sc0 sc1\n\t"
    "s_add_u32 s39, s39, 43008\n\t"
    "v_mfma_f32_16x16x32_bf16 v[92:95], v[140:143], v[64:67], v[92:95]\n\t"
    "s_waitcnt lgkmcnt(0)\n\t"
    "v_mfma_f32_16x16x32_bf16 v[88:91], v[160:163], v[76:79], v[88:91]\n\t"
    "v_mfma_f32_16x16x32_bf16 v[92:95], v[164:167], v[76:79], v[92:95]\n\t"
    "s_nop 6\n\t"
    "v_cvt_pk_bf16_f32 v88, v88, v89\n\t"
    "v_cvt_pk_bf16_f32 v89, v90, v91\n\t"
    "v_cvt_pk_bf16_f32 v90, v92, v93\n\t"
    "v_cvt_pk_bf16_f32 v91, v94, v95\n\t"
    "s_nop 0\n\t"
    "v_permlane16_swap_b32 v88, v90\n\t"
    "v_permlane16_swap_b32 v89, v91\n\t"
    "buffer_store_dwordx4 v[88:91], v4, s[24:27], s39 offen sc0 sc1\n\t"
    "L_finH:\n\t"
    "s_branch L_end\n\t"
    "L_lat:\n\t"
    "s_bitcmp1_b32 0x3c0, s30\n\t"
    "s_cselect_b32 s44, 14, 0\n\t"
    "s_xor_b32 s30, s30, s44\n\t"
    "s_lshl2_add_u32 s7, s30, 161280\n\t"
    "s_sub_u32 s5, s30, 4\n\t"
    "s_cmp_ge_u32 s30, 10\n\t"
    "s_cselect_b32 s44, 4, 0\n\t"
    "s_sub_u32 s37, s5, s44\n\t"
    "s_mov_b32 s43, 0x100000\n\t"
    "s_mov_b32 s44, 161280\n\t"
    "v_lshl_add_u32 v7, v1, 2, s44\n\t"
    "v_mov_b32 v9, s7\n\t"
    "v_lshrrev_b32 v16, 4, v1\n\t"
    "v_and_b32 v17, 7, v1\n\t"
    "v_bfe_u32 v18, v1, 3, 1\n\t"
    "v_mov_b32 v19, 196608\n\t"
    "v_mul_u32_u24 v20, 112, v16\n\t"
    "s_mov_b32 s44, 37632\n\t"
    "v_lshl_add_u32 v20, v17, 4, v20\n\t"
    "s_mov_b32 vcc_lo, 0x7f7f7f7f\n\t"
    "s_mov_b32 vcc_hi, vcc_lo\n\t"
    "v_mad_u32_u24 v3, v18, s44, v20\n\t"
    "v_bfe_u32 v21, v1, 2, 2\n\t"
    "v_lshl_add_u32 v21, v16, 3, v21\n\t"
    "v_mul_u32_u24 v21, 192, v21\n\t"
    "v_and_b32 v22, 3, v1\n\t"
    "v_lshl_add_u32 v123, v22, 4, v21\n\t"
    "v_and_b32 v23, 1, v16\n\t"
    "v_mul_u32_u24 v23, 1528, v23\n\t"
    "v_sub_u32 v123, v123, v23\n\t"
    "v_add_u32 v123, 142464, v123\n\t"
    "s_cmp_eq_u32 s5, 7\n\t"
    "s_cselect_b32 s4, 15, s5\n\t"
    "s_add_u32 s36, s5, 7\n\t"
    "s_add_u32 s40, s4, 1\n\t"
    "s_lshr_b32 s40, s40, 1\n\t"
    "s_lshl_b32 s40, s40, 11\n\t"
    "s_add_u32 s40, s40, 123648\n\t"
    "s_add_u32 s41, s36, 1\n\t"
    "s_lshr_b32 s41, s41, 1\n\t"
    "s_lshl_b32 s41, s41, 11\n\t"
    "s_add_u32 s41, s41, 123648\n\t"
    "s_sub_u32 s42, s41, s40\n\t"
    "v_mul_i32_i24 v23, s42, v18\n\t"
    "v_lshl_add_u32 v124, v16, 4, s40\n\t"
    "v_lshl_add_u32 v125, v16, 4, s41\n\t"
    "v_mul_u32_u24 v24, 192, v17\n\t"
    "v_sub_u32 v125, v125, v23\n\t"
    "v_add3_u32 v124, v124, v23, v24\n\t"
    "v_add_u32 v124, 192, v124\n\t"
    "v_cndmask_b32 v125, v125, v124, vcc\n\t"
    "s_mul_i32 s40, s4, 5376\n\t"
    "s_mul_i32 s41, s36, 5376\n\t"
    "s_sub_u32 s42, s41, s40\n\t"
    "v_mad_i32_i24 v0, s42, v18, v20\n\t"
    "v_add_u32 v0, s40, v0\n\t"
    "v_cndmask_b32 v0, v19, v0, vcc\n\t"
    "s_and_b32 s44, s4, 1\n\t"
    "s_lshl_b32 s44, s44, 6\n\t"
    "s_add_u32 s40, s40, s44\n\t"
    "s_xor_b32 s44, s44, 64\n\t"
    "s_add_u32 s41, s41, s44\n\t"
    "s_sub_u32 s42, s41, s40\n\t"
    "v_mul_i32_i24 v23, s42, v18\n\t"
    "v_lshrrev_b32 v20, 1, v17\n\t"
    "v_xor_b32 v20, v20, v16\n\t"
    "v_lshl_add_u32 v120, v20, 4, s40\n\t"
    "v_lshl_add_u32 v121, v20, 4, s41\n\t"
    "v_lshlrev_b32 v24, 1, v24\n\t"
    "v_sub_u32 v121, v121, v23\n\t"
    "v_add3_u32 v120, v120, v23, v24\n\t"
    "v_add_u32 v23, 384, v120\n\t"
    "v_cndmask_b32 v121, v121, v23, vcc\n\t"
    "s_lshl_b32 s44, s5, 1\n\t"
    "s_lshl_b32 s48, 3, s44\n\t"
    "s_mov_b32 s45, 0xffff0000\n\t"
    "s_cmp_ge_u32 s30, 10\n\t"
    "s_cbranch_scc1 L_E\n\t"
    "s_mul_i32 s44, s37, 448\n\t"
    "s_add_u32 s44, s44, 86016\n\t"
    "v_add_u32 v11, s44, v3\n\t"
    "v_cndmask_b32 v3, v19, v3, vcc\n\t"
    "v_cndmask_b32 v11, v19, v11, vcc\n\t"
    "v_xor_b32 v5, s37, v18\n\t"
    "v_bfe_u32 v20, v1, 2, 1\n\t"
    "v_lshl_add_u32 v20, v18, 3, v20\n\t"
    "v_mul_u32_u24 v20, 192, v20\n\t"
    "v_lshl_add_u32 v5, v5, 5, v20\n\t"
    "v_mul_u32_u24 v20, 6272, v16\n\t"
    "v_lshl_add_u32 v5, v22, 3, v5\n\t"
    "v_cmp_gt_u32 vcc, 48, v1\n\t"
    "s_mov_b32 s44, 86016\n\t"
    "v_add3_u32 v5, v5, v20, s44\n\t"
    "v_cndmask_b32 v5, v19, v5, vcc\n\t"
    "s_mov_b32 s46, 0xcccc0000\n\t"
    "s_mov_b32 s47, 0\n\t"
    "v_mov_b32 v18, 0x3f00\n\t"
    "v_mov_b32 v19, 0xbf00\n\t"
    "v_cndmask_b32 v18, v18, v19, s[46:47]\n\t"
    "v_and_b32 v19, 16, v2\n\t"
    "v_lshlrev_b32 v18, v19, v18\n\t"
    "s_mov_b32 s46, 0x000f000f\n\t"
    "s_mov_b32 s47, 15\n\t"
    "v_cndmask_b32 v12, 0, v18, s[46:47]\n\t"
    "s_lshl_b64 s[46:47], s[46:47], 4\n\t"
    "v_cndmask_b32 v13, 0, v18, s[46:47]\n\t"
    "s_lshl_b64 s[46:47], s[46:47], 4\n\t"
    "v_cndmask_b32 v14, 0, v18, s[46:47]\n\t"
    "s_lshl_b64 s[46:47], s[46:47], 4\n\t"
    "v_cndmask_b32 v15, 0, v18, s[46:47]\n\t"
    "s_cmp_ge_u32 s30, 6\n\t"
    "s_cbranch_scc1 L_G\n\t"
    "s_waitcnt lgkmcnt(0)\n\t"
    "s_barrier\n\t"
    "s_setprio 2\n\t"
    "L_poll29:\n\t"
    "ds_read_b32 v8, v7\n\t"
    "s_waitcnt lgkmcnt(0)\n\t"
    "v_cmp_lt_u32 vcc, 0, v8\n\t"
    "s_and_b32 s44, vcc_lo, 0xf000\n\t"
    "s_cbranch_scc0 L_polld29\n\t"
    "s_sub_u32 s43, s43, 1\n\t"
    "s_cbranch_scc1 L_end\n\t"
    "s_branch L_poll29\n\t"
    "L_polld29:\n\t"
    "ds_read_b64_tr_b16 v[16:17], v123 offset:0\n\t"
    "ds_read_b64_tr_b16 v[20:21], v123 offset:1536\n\t"
    "ds_read_b64_tr_b16 v[18:19], v123 offset:768\n\t"
    "ds_read_b64_tr_b16 v[22:23], v123 offset:2304\n\t"
    "ds_read_b64_tr_b16 v[24:25], v123 offset:6144\n\t"
    "ds_read_b64_tr_b16 v[28:29], v123 offset:7680\n\t"
    "ds_read_b64_tr_b16 v[26:27], v123 offset:6912\n\t"
    "ds_read_b64_tr_b16 v[30:31], v123 offset:8448\n\t"
    "ds_read_b64_tr_b16 v[32:33], v123 offset:12288\n\t"
    "ds_read_b64_tr_b16 v[36:37], v123 offset:13824\n\t"
    "ds_read_b64_tr_b16 v[34:35], v123 offset:13056\n\t"
    "ds_read_b64_tr_b16 v[38:39], v123 offset:14592\n\t"
    "ds_read_b64_tr_b16 v[40:41], v123 offset:64\n\t"
    "ds_read_b64_tr_b16 v[44:45], v123 offset:1600\n\t"
    "ds_read_b64_tr_b16 v[42:43], v123 offset:832\n\t"
    "ds_read_b64_tr_b16 v[46:47], v123 offset:2368\n\t"
    "ds_read_b64_tr_b16 v[48:49], v123 offset:6208\n\t"
    "ds_read_b64_tr_b16 v[52:53], v123 offset:7744\n\t"
    "ds_read_b64_tr_b16 v[50:51], v123 offset:6976\n\t"
    "ds_read_b64_tr_b16 v[54:55], v123 offset:8512\n\t"
    "ds_read_b64_tr_b16 v[56:57], v123 offset:12352\n\t"
    "ds_read_b64_tr_b16 v[60:61], v123 offset:13888\n\t"
    "ds_read_b64_tr_b16 v[58:59], v123 offset:13120\n\t"
    "ds_read_b64_tr_b16 v[62:63], v123 offset:14656\n\t"
    "ds_read_b64_tr_b16 v[64:65], v123 offset:128\n\t"
    "ds_read_b64_tr_b16 v[68:69], v123 offset:1664\n\t"
    "ds_read_b64_tr_b16 v[66:67], v123 offset:896\n\t"
    "ds_read_b64_tr_b16 v[70:71], v123 offset:2432\n\t"
    "ds_read_b64_tr_b16 v[72:73], v123 offset:6272\n\t"
    "ds_read_b64_tr_b16 v[76:77], v123 offset:7808\n\t"
    "ds_read_b64_tr_b16 v[74:75], v123 offset:7040\n\t"
    "ds_read_b64_tr_b16 v[78:79], v123 offset:8576\n\t"
    "ds_read_b64_tr_b16 v[80:81], v123 offset:12416\n\t"
    "ds_read_b64_tr_b16 v[84:85], v123 offset:13952\n\t"
    "ds_read_b64_tr_b16 v[82:83], v123 offset:13184\n\t"
    "ds_read_b64_tr_b16 v[86:87], v123 offset:14720\n\t"
    "s_waitcnt lgkmcnt(15)\n\t"
    "v_permlane16_swap_b32 v16, v20\n\t"
    "v_permlane16_swap_b32 v17, v21\n\t"
    "v_permlane16_swap_b32 v18, v22\n\t"
    "v_permlane16_swap_b32 v19, v23\n\t"
    "v_permlane16_swap_b32 v24, v28\n\t"
    "v_permlane16_swap_b32 v25, v29\n\t"
    "v_permlane16_swap_b32 v26, v30\n\t"
    "v_permlane16_swap_b32 v27, v31\n\t"
    "v_permlane16_swap_b32 v32, v36\n\t"
    "v_permlane16_swap_b32 v33, v37\n\t"
    "v_permlane16_swap_b32 v34, v38\n\t"
    "v_permlane16_swap_b32 v35, v39\n\t"
    "v_permlane16_swap_b32 v40, v44\n\t"
    "v_permlane16_swap_b32 v41, v45\n\t"
    "v_permlane16_swap_b32 v42, v46\n\t"
    "v_permlane16_swap_b32 v43, v47\n\t"
    "v_permlane16_swap_b32 v48, v52\n\t"
    "v_permlane16_swap_b32 v49, v53\n\t"
    "v_permlane16_swap_b32 v50, v54\n\t"
    "v_permlane16_swap_b32 v51, v55\n\t"
    "s_waitcnt lgkmcnt(14)\n\t"
    "v_permlane16_swap_b32 v56, v60\n\t"
    "v_permlane16_swap_b32 v57, v61\n\t"
    "s_waitcnt lgkmcnt(12)\n\t"
    "v_permlane16_swap_b32 v58, v62\n\t"
    "v_permlane16_swap_b32 v59, v63\n\t"
    "s_waitcnt lgkmcnt(10)\n\t"
    "v_permlane16_swap_b32 v64, v68\n\t"
    "v_permlane16_swap_b32 v65, v69\n\t"
    "s_waitcnt lgkmcnt(8)\n\t"
    "v_permlane16_swap_b32 v66, v70\n\t"
    "v_permlane16_swap_b32 v67, v71\n\t"
    "s_waitcnt lgkmcnt(6)\n\t"
    "v_permlane16_swap_b32 v72, v76\n\t"
    "v_permlane16_swap_b32 v73, v77\n\t"
    "s_waitcnt lgkmcnt(4)\n\t"
    "v_permlane16_swap_b32 v74, v78\n\t"
    "v_permlane16_swap_b32 v75, v79\n\t"
    "s_waitcnt lgkmcnt(2)\n\t"
    "v_permlane16_swap_b32 v80, v84\n\t"
    "v_permlane16_swap_b32 v81, v85\n\t"
    "s_waitcnt lgkmcnt(0)\n\t"
    "v_permlane16_swap_b32 v82, v86\n\t"
    "v_permlane16_swap_b32 v83, v87\n\t"
    "ds_read_b32 v8, v7\n\t"
    "ds_read_b128 v[156:159], v124 offset:0\n\t"
    "ds_read_b128 v[164:167], v125 offset:0\n\t"
    "ds_read_b128 v[88:91], v124 offset:64\n\t"
    "ds_read_b128 v[92:95], v125 offset:64\n\t"
    "ds_read_b128 v[112:115], v124 offset:128\n\t"
    "ds_read_b128 v[116:119], v125 offset:128\n\t"
    "s_waitcnt lgkmcnt(6)\n\t"
    "v_cmp_lt_u32 vcc, 0, v8\n\t"
    "s_and_b32 s44, vcc_lo, 0xf0000\n\t"
    "s_cbranch_scc1 L_poll30\n\t"
    "L_polld30:\n\t"
    "v_mov_b32 v10, 9\n\t"
    "ds_write_b32 v9, v10\n\t"
    "s_waitcnt lgkmcnt(6)\n\t"
    "v_lshlrev_b32 v152, 16, v156\n\t"
    "v_and_b32 v153, s45, v156\n\t"
    "v_lshlrev_b32 v154, 16, v157\n\t"
    "v_and_b32 v155, s45, v157\n\t"
    "v_lshlrev_b32 v156, 16, v158\n\t"
    "v_and_b32 v157, s45, v158\n\t"
    "v_lshlrev_b32 v158, 16, v159\n\t"
    "v_and_b32 v159, s45, v159\n\t"
    "s_waitcnt lgkmcnt(5)\n\t"
    "v_lshlrev_b32 v160, 16, v164\n\t"
    "v_and_b32 v161, s45, v164\n\t"
    "v_lshlrev_b32 v162, 16, v165\n\t"
    "v_and_b32 v163, s45, v165\n\t"
    "v_lshlrev_b32 v164, 16, v166\n\t"
    "v_and_b32 v165, s45, v166\n\t"
    "v_lshlrev_b32 v166, 16, v167\n\t"
    "v_and_b32 v167, s45, v167\n\t"
    "ds_read_b32 v8, v7 offset:256\n\t"
    "ds_read_b128 v[128:131], v120 offset:192\n\t"
    "ds_read_b128 v[140:143], v121 offset:0\n\t"
    "ds_read_b128 v[132:135], v120 offset:256\n\t"
    "ds_read_b128 v[144:147], v121 offset:64\n\t"
    "ds_read_b128 v[136:139], v120 offset:320\n\t"
    "ds_read_b128 v[148:151], v121 offset:128\n\t"
    "s_waitcnt lgkmcnt(6)\n\t"
    "v_cmp_lt_u32 vcc, 0, v8\n\t"
    "s_and_b32 s44, vcc_lo, s48\n\t"
    "s_cbranch_scc1 L_poll31\n\t"
    "L_polld31:\n\t"
    "s_nop 0\n\t"
    "s_waitcnt lgkmcnt(5)\n\t"
    "v_mfma_f32_16x16x32_bf16 v[152:155], v[16:19], v[128:131], v[152:155]\n\t"
    "v_mfma_f32_16x16x32_bf16 v[156:159], v[20:23], v[128:131], v[156:159]\n\t"
    "s_waitcnt lgkmcnt(4)\n\t"
    "v_mfma_f32_16x16x32_bf16 v[160:163], v[16:19], v[140:143], v[160:163]\n\t"
    "v_mfma_f32_16x16x32_bf16 v[164:167], v[20:23], v[140:143], v[164:167]\n\t"
    "s_waitcnt lgkmcnt(3)\n\t"
    "v_mfma_f32_16x16x32_bf16 v[152:155], v[24:27], v[132:135], v[152:155]\n\t"
    "v_lshlrev_b32 v16, 16, v88\n\t"
    "v_and_b32 v17, s45, v88\n\t"
    "v_lshlrev_b32 v18, 16, v89\n\t"
    "v_mfma_f32_16x16x32_bf16 v[156:159], v[28:31], v[132:135], v[156:159]\n\t"
    "v_and_b32 v19, s45, v89\n\t"
    "v_lshlrev_b32 v20, 16, v90\n\t"
    "v_and_b32 v21, s45, v90\n\t"
    "s_waitcnt lgkmcnt(2)\n\t"
    "v_mfma_f32_16x16x32_bf16 v[160:163], v[24:27], v[144:147], v[160:163]\n\t"
    "v_lshlrev_b32 v22, 16, v91\n\t"
    "v_and_b32 v23, s45, v91\n\t"
    "v_mfma_f32_16x16x32_bf16 v[164:167], v[28:31], v[144:147], v[164:167]\n\t"
    "s_waitcnt lgkmcnt(0)\n\t"
    "v_mfma_f32_16x16x32_bf16 v[152:155], v[32:35], v[136:139], v[152:155]\n\t"
    "v_lshlrev_b32 v24, 16, v92\n\t"
    "v_and_b32 v25, s45, v92\n\t"
    "v_lshlrev_b32 v26, 16, v93\n\t"
    "v_mfma_f32_16x16x32_bf16 v[156:159], v[36:39], v[136:139], v[156:159]\n\t"
    "v_and_b32 v27, s45, v93\n\t"
    "v_lshlrev_b32 v28, 16, v94\n\t"
    "v_and_b32 v29, s45, v94\n\t"
    "v_mfma_f32_16x16x32_bf16 v[160:163], v[32:35], v[148:151], v[160:163]\n\t"
    "v_lshlrev_b32 v30, 16, v95\n\t"
    "v_and_b32 v31, s45, v95\n\t"
    "v_mfma_f32_16x16x32_bf16 v[164:167], v[36:39], v[148:151], v[164:167]\n\t"
    "v_mfma_f32_16x16x32_bf16 v[16:19], v[40:43], v[128:131], v[16:19]\n\t"
    "s_nop 3\n\t"
    "v_add_f32 v92, v160, v152\n\t"
    "v_add_f32 v93, v161, v153\n\t"
    "v_cvt_pk_bf16_f32 v88, v92, v93\n\t"
    "v_add_f32 v94, v162, v154\n\t"
    "v_mfma_f32_16x16x32_bf16 v[20:23], v[44:47], v[128:131], v[20:23]\n\t"
    "v_add_f32 v95, v163, v155\n\t"
    "v_cvt_pk_bf16_f32 v89, v94, v95\n\t"
    "v_add_f32 v92, v164, v156\n\t"
    "v_add_f32 v93, v165, v157\n\t"
    "v_mfma_f32_16x16x32_bf16 v[24:27], v[40:43], v[140:143], v[24:27]\n\t"
    "v_cvt_pk_bf16_f32 v90, v92, v93\n\t"
    "v_add_f32 v94, v166, v158\n\t"
    "v_add_f32 v95, v167, v159\n\t"
    "v_cvt_pk_bf16_f32 v91, v94, v95\n\t"
    "v_mfma_f32_16x16x32_bf16 v[28:31], v[44:47], v[140:143], v[28:31]\n\t"
    "ds_write_b128 v0, v[88:91] offset:0\n\t"
    "v_lshlrev_b32 v32, 16, v112\n\t"
    "v_and_b32 v33, s45, v112\n\t"
    "v_mfma_f32_16x16x32_bf16 v[16:19], v[48:51], v[132:135], v[16:19]\n\t"
    "v_lshlrev_b32 v34, 16, v113\n\t"
    "v_and_b32 v35, s45, v113\n\t"
    "v_lshlrev_b32 v36, 16, v114\n\t"
    "v_and_b32 v37, s45, v114\n\t"
    "v_mfma_f32_16x16x32_bf16 v[20:23], v[52:55], v[132:135], v[20:23]\n\t"
    "v_lshlrev_b32 v38, 16, v115\n\t"
    "v_and_b32 v39, s45, v115\n\t"
    "v_lshlrev_b32 v40, 16, v116\n\t"
    "v_and_b32 v41, s45, v116\n\t"
    "v_mfma_f32_16x16x32_bf16 v[24:27], v[48:51], v[144:147], v[24:27]\n\t"
    "v_lshlrev_b32 v42, 16, v117\n\t"
    "v_and_b32 v43, s45, v117\n\t"
    "v_lshlrev_b32 v44, 16, v118\n\t"
    "v_and_b32 v45, s45, v118\n\t"
    "v_mfma_f32_16x16x32_bf16 v[28:31], v[52:55], v[144:147], v[28:31]\n\t"
    "v_lshlrev_b32 v46, 16, v119\n\t"
    "v_and_b32 v47, s45, v119\n\t"
    "v_sub_f32 v93, v160, v152\n\t"
    "v_sub_f32 v94, v161, v153\n\t"
    "v_mfma_f32_16x16x32_bf16 v[16:19], v[56:59], v[136:139], v[16:19]\n\t"
    "v_cvt_pk_bf16_f32 v92, v93, v94\n\t"
    "v_sub_f32 v94, v162, v154\n\t"
    "v_sub_f32 v95, v163, v155\n\t"
    "v_cvt_pk_bf16_f32 v93, v94, v95\n\t"
    "v_mfma_f32_16x16x32_bf16 v[20:23], v[60:63], v[136:139], v[20:23]\n\t"
    "v_sub_f32 v94, v164, v156\n\t"
    "v_sub_f32 v95, v165, v157\n\t"
    "v_cvt_pk_bf16_f32 v94, v94, v95\n\t"
    "v_sub_f32 v95, v166, v158\n\t"
    "v_mfma_f32_16x16x32_bf16 v[24:27], v[56:59], v[148:151], v[24:27]\n\t"
    "v_sub_f32 v10, v167, v159\n\t"
    "v_cvt_pk_bf16_f32 v95, v95, v10\n\t"
    "ds_write_b128 v0, v[92:95] offset:1344\n\t"
    "v_mfma_f32_16x16x32_bf16 v[28:31], v[60:63], v[148:151], v[28:31]\n\t"
    "v_mov_b32 v10, 3\n\t"
    "ds_write_b32 v9, v10 offset:208\n\t"
    "v_mfma_f32_16x16x32_bf16 v[32:35], v[64:67], v[128:131], v[32:35]\n\t"
    "s_nop 0\n\t"
    "v_add_f32 v92, v24, v16\n\t"
    "v_add_f32 v93, v25, v17\n\t"
    "v_cvt_pk_bf16_f32 v88, v92, v93\n\t"
    "v_add_f32 v94, v26, v18\n\t"
    "v_mfma_f32_16x16x32_bf16 v[36:39], v[68:71], v[128:131], v[36:39]\n\t"
    "v_add_f32 v95, v27, v19\n\t"
    "v_cvt_pk_bf16_f32 v89, v94, v95\n\t"
    "v_add_f32 v92, v28, v20\n\t"
    "v_add_f32 v93, v29, v21\n\t"
    "v_mfma_f32_16x16x32_bf16 v[40:43], v[64:67], v[140:143], v[40:43]\n\t"
    "v_cvt_pk_bf16_f32 v90, v92, v93\n\t"
    "v_add_f32 v94, v30, v22\n\t"
    "v_add_f32 v95, v31, v23\n\t"
    "v_cvt_pk_bf16_f32 v91, v94, v95\n\t"
    "v_mfma_f32_16x16x32_bf16 v[44:47], v[68:71], v[140:143], v[44:47]\n\t"
    "ds_write_b128 v0, v[88:91] offset:448\n\t"
    "v_sub_f32 v93, v24, v16\n\t"
    "v_sub_f32 v94, v25, v17\n\t"
    "v_mfma_f32_16x16x32_bf16 v[32:35], v[72:75], v[132:135], v[32:35]\n\t"
    "v_cvt_pk_bf16_f32 v92, v93, v94\n\t"
    "v_sub_f32 v94, v26, v18\n\t"
    "v_sub_f32 v95, v27, v19\n\t"
    "v_cvt_pk_bf16_f32 v93, v94, v95\n\t"
    "v_mfma_f32_16x16x32_bf16 v[36:39], v[76:79], v[132:135], v[36:39]\n\t"
    "v_sub_f32 v94, v28, v20\n\t"
    "v_sub_f32 v95, v29, v21\n\t"
    "v_cvt_pk_bf16_f32 v94, v94, v95\n\t"
    "v_sub_f32 v95, v30, v22\n\t"
    "v_mfma_f32_16x16x32_bf16 v[40:43], v[72:75], v[144:147], v[40:43]\n\t"
    "v_sub_f32 v10, v31, v23\n\t"
    "v_cvt_pk_bf16_f32 v95, v95, v10\n\t"
    "ds_write_b128 v0, v[92:95] offset:1792\n\t"
    "v_mfma_f32_16x16x32_bf16 v[44:47], v[76:79], v[144:147], v[44:47]\n\t"
    "v_mfma_f32_16x16x32_bf16 v[32:35], v[80:83], v[136:139], v[32:35]\n\t"
    "v_mfma_f32_16x16x32_bf16 v[36:39], v[84:87], v[136:139], v[36:39]\n\t"
    "v_mfma_f32_16x16x32_bf16 v[40:43], v[80:83], v[148:151], v[40:43]\n\t"
    "v_mfma_f32_16x16x32_bf16 v[44:47], v[84:87], v[148:151], v[44:47]\n\t"
    "s_nop 6\n\t"
    "v_add_f32 v92, v40, v32\n\t"
    "v_add_f32 v93, v41, v33\n\t"
    "v_cvt_pk_bf16_f32 v88, v92, v93\n\t"
    "v_add_f32 v94, v42, v34\n\t"
    "v_add_f32 v95, v43, v35\n\t"
    "v_cvt_pk_bf16_f32 v89, v94, v95\n\t"
    "v_add_f32 v92, v44, v36\n\t"
    "v_add_f32 v93, v45, v37\n\t"
    "v_cvt_pk_bf16_f32 v90, v92, v93\n\t"
    "v_add_f32 v94, v46, v38\n\t"
    "v_add_f32 v95, v47, v39\n\t"
    "v_cvt_pk_bf16_f32 v91, v94, v95\n\t"
    "ds_write_b128 v0, v[88:91] offset:896\n\t"
    "v_sub_f32 v93, v40, v32\n\t"
    "v_sub_f32 v94, v41, v33\n\t"
    "v_cvt_pk_bf16_f32 v92, v93, v94\n\t"
    "v_sub_f32 v94, v42, v34\n\t"
    "v_sub_f32 v95, v43, v35\n\t"
    "v_cvt_pk_bf16_f32 v93, v94, v95\n\t"
    "v_sub_f32 v94, v44, v36\n\t"
    "v_sub_f32 v95, v45, v37\n\t"
    "v_cvt_pk_bf16_f32 v94, v94, v95\n\t"
    "v_sub_f32 v95, v46, v38\n\t"
    "v_sub_f32 v10, v47, v39\n\t"
    "v_cvt_pk_bf16_f32 v95, v95, v10\n\t"
    "ds_write_b128 v0, v[92:95] offset:2240\n\t"
    "v_mov_b32 v10, 1\n\t"
    "ds_write_b32 v9, v10 offset:208\n\t"
    "s_waitcnt vmcnt(0)\n\t"
    "v_sub_f32_dpp v40, v40, v40 row_ror:1 row_mask:0xf bank_mask:0xf bound_ctrl:0\n\t"
    "v_sub_f32_dpp v41, v41, v41 row_ror:1 row_mask:0xf bank_mask:0xf bound_ctrl:0\n\t"
    "v_sub_f32_dpp v42, v42, v42 row_ror:1 row_mask:0xf bank_mask:0xf bound_ctrl:0\n\t"
    "v_sub_f32_dpp v43, v43, v43 row_ror:1 row_mask:0xf bank_mask:0xf bound_ctrl:0\n\t"
    "v_sub_f32_dpp v44, v44, v44 row_ror:1 row_mask:0xf bank_mask:0xf bound_ctrl:0\n\t"
    "v_sub_f32_dpp v45, v45, v45 row_ror:1 row_mask:0xf bank_mask:0xf bound_ctrl:0\n\t"
    "v_sub_f32_dpp v46, v46, v46 row_ror:1 row_mask:0xf bank_mask:0xf bound_ctrl:0\n\t"
    "v_sub_f32_dpp v47, v47, v47 row_ror:1 row_mask:0xf bank_mask:0xf bound_ctrl:0\n\t"
    "v_cvt_pk_bf16_f32 v40, v40, v41\n\t"
    "v_cvt_pk_bf16_f32 v41, v42, v43\n\t"
    "v_cvt_pk_bf16_f32 v42, v44, v45\n\t"
    "v_cvt_pk_bf16_f32 v43, v46, v47\n\t"
    "ds_write_b128 v0, v[40:43] offset:3584\n\t"
    "v_sub_f32_dpp v32, v32, v32 row_shl:1 row_mask:0xf bank_mask:0xf bound_ctrl:0\n\t"
    "v_sub_f32_dpp v33, v33, v33 row_shl:1 row_mask:0xf bank_mask:0xf bound_ctrl:0\n\t"
    "v_sub_f32_dpp v34, v34, v34 row_shl:1 row_mask:0xf bank_mask:0xf bound_ctrl:0\n\t"
    "v_sub_f32_dpp v35, v35, v35 row_shl:1 row_mask:0xf bank_mask:0xf bound_ctrl:0\n\t"
    "v_sub_f32_dpp v36, v36, v36 row_shl:1 row_mask:0xf bank_mask:0xf bound_ctrl:0\n\t"
    "v_sub_f32_dpp v37, v37, v37 row_shl:1 row_mask:0xf bank_mask:0xf bound_ctrl:0\n\t"
    "v_sub_f32_dpp v38, v38, v38 row_shl:1 row_mask:0xf bank_mask:0xf bound_ctrl:0\n\t"
    "v_sub_f32_dpp v39, v39, v39 row_shl:1 row_mask:0xf bank_mask:0xf bound_ctrl:0\n\t"
    "v_cvt_pk_bf16_f32 v32, v32, v33\n\t"
    "v_cvt_pk_bf16_f32 v33, v34, v35\n\t"
    "v_cvt_pk_bf16_f32 v34, v36, v37\n\t"
    "v_cvt_pk_bf16_f32 v35, v38, v39\n\t"
    "ds_write_b128 v0, v[32:35] offset:4928\n\t"
    "v_sub_f32_dpp v24, v24, v24 row_ror:1 row_mask:0xf bank_mask:0xf bound_ctrl:0\n\t"
    "v_sub_f32_dpp v25, v25, v25 row_ror:1 row_mask:0xf bank_mask:0xf bound_ctrl:0\n\t"
    "v_sub_f32_dpp v26, v26, v26 row_ror:1 row_mask:0xf bank_mask:0xf bound_ctrl:0\n\t"
    "v_sub_f32_dpp v27, v27, v27 row_ror:1 row_mask:0xf bank_mask:0xf bound_ctrl:0\n\t"
    "v_sub_f32_dpp v28, v28, v28 row_ror:1 row_mask:0xf bank_mask:0xf bound_ctrl:0\n\t"
    "v_sub_f32_dpp v29, v29, v29 row_ror:1 row_mask:0xf bank_mask:0xf bound_ctrl:0\n\t"
    "v_sub_f32_dpp v30, v30, v30 row_ror:1 row_mask:0xf bank_mask:0xf bound_ctrl:0\n\t"
    "v_sub_f32_dpp v31, v31, v31 row_ror:1 row_mask:0xf bank_mask:0xf bound_ctrl:0\n\t"
    "v_cvt_pk_bf16_f32 v24, v24, v25\n\t"
    "v_cvt_pk_bf16_f32 v25, v26, v27\n\t"
    "v_cvt_pk_bf16_f32 v26, v28, v29\n\t"
    "v_cvt_pk_bf16_f32 v27, v30, v31\n\t"
    "ds_write_b128 v0, v[24:27] offset:3136\n\t"
    "v_sub_f32_dpp v16, v16, v16 row_shl:1 row_mask:0xf bank_mask:0xf bound_ctrl:0\n\t"
    "v_sub_f32_dpp v17, v17, v17 row_shl:1 row_mask:0xf bank_mask:0xf bound_ctrl:0\n\t"
    "v_sub_f32_dpp v18, v18, v18 row_shl:1 row_mask:0xf bank_mask:0xf bound_ctrl:0\n\t"
    "v_sub_f32_dpp v19, v19, v19 row_shl:1 row_mask:0xf bank_mask:0xf bound_ctrl:0\n\t"
    "v_sub_f32_dpp v20, v20, v20 row_shl:1 row_mask:0xf bank_mask:0xf bound_ctrl:0\n\t"
    "v_sub_f32_dpp v21, v21, v21 row_shl:1 row_mask:0xf bank_mask:0xf bound_ctrl:0\n\t"
    "v_sub_f32_dpp v22, v22, v22 row_shl:1 row_mask:0xf bank_mask:0xf bound_ctrl:0\n\t"
    "v_sub_f32_dpp v23, v23, v23 row_shl:1 row_mask:0xf bank_mask:0xf bound_ctrl:0\n\t"
    "v_cvt_pk_bf16_f32 v16, v16, v17\n\t"
    "v_cvt_pk_bf16_f32 v17, v18, v19\n\t"
    "v_cvt_pk_bf16_f32 v18, v20, v21\n\t"
    "v_cvt_pk_bf16_f32 v19, v22, v23\n\t"
    "ds_write_b128 v0, v[16:19] offset:4480\n\t"
    "v_sub_f32_dpp v160, v160, v160 row_ror:1 row_mask:0xf bank_mask:0xf bound_ctrl:0\n\t"
    "v_sub_f32_dpp v161, v161, v161 row_ror:1 row_mask:0xf bank_mask:0xf bound_ctrl:0\n\t"
    "v_sub_f32_dpp v162, v162, v162 row_ror:1 row_mask:0xf bank_mask:0xf bound_ctrl:0\n\t"
    "v_sub_f32_dpp v163, v163, v163 row_ror:1 row_mask:0xf bank_mask:0xf bound_ctrl:0\n\t"
    "v_sub_f32_dpp v164, v164, v164 row_ror:1 row_mask:0xf bank_mask:0xf bound_ctrl:0\n\t"
    "v_sub_f32_dpp v165, v165, v165 row_ror:1 row_mask:0xf bank_mask:0xf bound_ctrl:0\n\t"
    "v_sub_f32_dpp v166, v166, v166 row_ror:1 row_mask:0xf bank_mask:0xf bound_ctrl:0\n\t"
    "v_sub_f32_dpp v167, v167, v167 row_ror:1 row_mask:0xf bank_mask:0xf bound_ctrl:0\n\t"
    "v_cvt_pk_bf16_f32 v160, v160, v161\n\t"
    "v_cvt_pk_bf16_f32 v161, v162, v163\n\t"
    "v_cvt_pk_bf16_f32 v162, v164, v165\n\t"
    "v_cvt_pk_bf16_f32 v163, v166, v167\n\t"
    "ds_write_b128 v0, v[160:163] offset:2688\n\t"
    "ds_read_b32 v8, v7\n\t"
    "v_sub_f32_dpp v152, v152, v152 row_shl:1 row_mask:0xf bank_mask:0xf bound_ctrl:0\n\t"
    "v_sub_f32_dpp v153, v153, v153 row_shl:1 row_mask:0xf bank_mask:0xf bound_ctrl:0\n\t"
    "v_sub_f32_dpp v154, v154, v154 row_shl:1 row_mask:0xf bank_mask:0xf bound_ctrl:0\n\t"
    "v_sub_f32_dpp v155, v155, v155 row_shl:1 row_mask:0xf bank_mask:0xf bound_ctrl:0\n\t"
    "v_sub_f32_dpp v156, v156, v156 row_shl:1 row_mask:0xf bank_mask:0xf bound_ctrl:0\n\t"
    "v_sub_f32_dpp v157, v157, v157 row_shl:1 row_mask:0xf bank_mask:0xf bound_ctrl:0\n\t"
    "v_sub_f32_dpp v158, v158, v158 row_shl:1 row_mask:0xf bank_mask:0xf bound_ctrl:0\n\t"
    "v_sub_f32_dpp v159, v159, v159 row_shl:1 row_mask:0xf bank_mask:0xf bound_ctrl:0\n\t"
    "v_cvt_pk_bf16_f32 v152, v152, v153\n\t"
    "v_cvt_pk_bf16_f32 v153, v154, v155\n\t"
    "v_cvt_pk_bf16_f32 v154, v156, v157\n\t"
    "v_cvt_pk_bf16_f32 v155, v158, v159\n\t"
    "ds_write_b128 v0, v[152:155] offset:4032\n\t"
    "v_mov_b32 v10, 0\n\t"
    "ds_write_b32 v9, v10 offset:208\n\t"
    "s_waitcnt lgkmcnt(2)\n\t"
    "v_cmp_lt_u32 vcc, 0, v8\n\t"
    "s_and_b32 s44, vcc_lo, 0xf00000\n\t"
    "s_cbranch_scc1 L_slow32\n\t"
    "L_cont32:\n\t"
    "ds_read_b64_tr_b16 v[104:105], v5 offset:0\n\t"
    "ds_read_b64_tr_b16 v[106:107], v5 offset:3072\n\t"
    "ds_read_b64_tr_b16 v[108:109], v5 offset:384\n\t"
    "ds_read_b64_tr_b16 v[110:111], v5 offset:3456\n\t"
    "ds_read_b64_tr_b16 v[116:117], v5 offset:768\n\t"
    "ds_read_b64_tr_b16 v[118:119], v5 offset:3840\n\t"
    "ds_read_b64_tr_b16 v[112:113], v5 offset:1152\n\t"
    "ds_read_b64_tr_b16 v[114:115], v5 offset:4224\n\t"
    "v_mov_b32 v10, 8\n\t"
    "ds_write_b32 v9, v10\n\t"
    "v_cmp_lt_u32 vcc, 0, v8\n\t"
    "s_waitcnt lgkmcnt(0)\n\t"
    "v_mfma_f32_16x16x32_bf16 v[104:107], v[12:15], v[104:107], 0\n\t"
    "s_and_b32 s44, vcc_lo, 0xf000000\n\t"
    "s_cbranch_scc1 L_slow33\n\t"
    "L_cont33:\n\t"
    "ds_read_b64_tr_b16 v[40:41], v5 offset:18816\n\t"
    "v_mfma_f32_16x16x32_bf16 v[108:111], v[12:15], v[108:111], 0\n\t"
    "ds_read_b64_tr_b16 v[42:43], v5 offset:21888\n\t"
    "ds_read_b64_tr_b16 v[44:45], v5 offset:19200\n\t"
    "ds_read_b64_tr_b16 v[46:47], v5 offset:22272\n\t"
    "v_mfma_f32_16x16x32_bf16 v[116:119], v[12:15], v[116:119], 0\n\t"
    "v_cvt_pk_bf16_f32 v96, v104, v105\n\t"
    "v_cvt_pk_bf16_f32 v100, v106, v107\n\t"
    "ds_read_b64_tr_b16 v[48:49], v5 offset:19584\n\t"
    "v_mfma_f32_16x16x32_bf16 v[112:115], v[12:15], v[112:115], 0\n\t"
    "v_cvt_pk_bf16_f32 v97, v108, v109\n\t"
    "v_cvt_pk_bf16_f32 v101, v110, v111\n\t"
    "ds_read_b64_tr_b16 v[50:51], v5 offset:22656\n\t"
    "s_nop 0\n\t"
    "v_cvt_pk_bf16_f32 v98, v116, v117\n\t"
    "v_cvt_pk_bf16_f32 v102, v118, v119\n\t"
    "ds_read_b64_tr_b16 v[52:53], v5 offset:19968\n\t"
    "s_nop 0\n\t"
    "v_cvt_pk_bf16_f32 v99, v112, v113\n\t"
    "v_cvt_pk_bf16_f32 v103, v114, v115\n\t"
    "ds_read_b64_tr_b16 v[54:55], v5 offset:23040\n\t"
    "v_mov_b32 v10, 7\n\t"
    "ds_write_b32 v9, v10\n\t"
    "v_cmp_lt_u32 vcc, 0, v8\n\t"
    "s_waitcnt lgkmcnt(3)\n\t"
    "v_mfma_f32_16x16x32_bf16 v[40:43], v[12:15], v[40:43], 0\n\t"
    "s_and_b32 s44, vcc_lo, 0xf0000000\n\t"
    "s_cbranch_scc1 L_slow34\n\t"
    "L_cont34:\n\t"
    "ds_read_b64_tr_b16 v[56:57], v5 offset:37632\n\t"
    "v_mfma_f32_16x16x32_bf16 v[44:47], v[12:15], v[44:47], 0\n\t"
    "ds_read_b64_tr_b16 v[58:59], v5 offset:40704\n\t"
    "ds_read_b64_tr_b16 v[60:61], v5 offset:38016\n\t"
    "ds_read_b64_tr_b16 v[62:63], v5 offset:41088\n\t"
    "v_mfma_f32_16x16x32_bf16 v[48:51], v[12:15], v[48:51], 0\n\t"
    "v_cvt_pk_bf16_f32 v104, v40, v41\n\t"
    "v_cvt_pk_bf16_f32 v108, v42, v43\n\t"
    "ds_read_b64_tr_b16 v[64:65], v5 offset:38400\n\t"
    "s_waitcnt lgkmcnt(5)\n\t"
    "v_mfma_f32_16x16x32_bf16 v[52:55], v[12:15], v[52:55], 0\n\t"
    "v_cvt_pk_bf16_f32 v105, v44, v45\n\t"
    "v_cvt_pk_bf16_f32 v109, v46, v47\n\t"
    "ds_read_b64_tr_b16 v[66:67], v5 offset:41472\n\t"
    "v_cvt_pk_bf16_f32 v106, v48, v49\n\t"
    "v_cvt_pk_bf16_f32 v110, v50, v51\n\t"
    "ds_read_b64_tr_b16 v[68:69], v5 offset:38784\n\t"
    "s_nop 1\n\t"
    "v_cvt_pk_bf16_f32 v107, v52, v53\n\t"
    "v_cvt_pk_bf16_f32 v111, v54, v55\n\t"
    "ds_read_b64_tr_b16 v[70:71], v5 offset:41856\n\t"
    "v_cmp_lt_u32 vcc, 3, v8\n\t"
    "v_mov_b32 v10, 6\n\t"
    "ds_write_b32 v9, v10\n\t"
    "s_and_b32 s44, vcc_hi, 0x3f000000\n\t"
    "s_cbranch_scc1 L_slow35\n\t"
    "L_cont35:\n\t"
    "ds_read_b128 v[44:47], v3 offset:0\n\t"
    "ds_read_b128 v[48:51], v3 offset:1344\n\t"
    "ds_read_b128 v[52:55], v3 offset:5376\n\t"
    "ds_read_b128 v[80:83], v3 offset:6720\n\t"
    "s_waitcnt lgkmcnt(7)\n\t"
    "v_mfma_f32_16x16x32_bf16 v[56:59], v[12:15], v[56:59], 0\n\t"
    "v_mfma_f32_16x16x32_bf16 v[60:63], v[12:15], v[60:63], 0\n\t"
    "ds_read_b128 v[84:87], v3 offset:10752\n\t"
    "v_mfma_f32_16x16x32_bf16 v[64:67], v[12:15], v[64:67], 0\n\t"
    "s_nop 4\n\t"
    "v_cvt_pk_bf16_f32 v112, v56, v57\n\t"
    "v_cvt_pk_bf16_f32 v116, v58, v59\n\t"
    "s_waitcnt lgkmcnt(5)\n\t"
    "v_mfma_f32_16x16x32_bf16 v[68:71], v[12:15], v[68:71], 0\n\t"
    "v_cvt_pk_bf16_f32 v113, v60, v61\n\t"
    "v_cvt_pk_bf16_f32 v117, v62, v63\n\t"
    "s_setprio 3\n\t"
    "s_waitcnt lgkmcnt(4)\n\t"
    "v_mfma_f32_16x16x32_bf16 v[40:43], v[96:99], v[44:47], 0\n\t"
    "v_cvt_pk_bf16_f32 v114, v64, v65\n\t"
    "v_cvt_pk_bf16_f32 v118, v66, v67\n\t"
    "ds_read_b128 v[72:75], v3 offset:12096\n\t"
    "s_waitcnt lgkmcnt(4)\n\t"
    "v_mfma_f32_16x16x32_bf16 v[44:47], v[100:103], v[48:51], 0\n\t"
    "v_cvt_pk_bf16_f32 v115, v68, v69\n\t"
    "v_cvt_pk_bf16_f32 v119, v70, v71\n\t"
    "s_waitcnt lgkmcnt(3)\n\t"
    "v_mfma_f32_16x16x32_bf16 v[40:43], v[104:107], v[52:55], v[40:43]\n\t"
    "ds_read_b32 v8, v7\n\t"
    "v_mfma_f32_16x16x32_bf16 v[48:51], v[96:99], v[52:55], 0\n\t"
    "ds_read_b128 v[88:91], v3 offset:16128\n\t"
    "s_waitcnt lgkmcnt(4)\n\t"
    "v_mfma_f32_16x16x32_bf16 v[44:47], v[108:111], v[80:83], v[44:47]\n\t"
    "v_mfma_f32_16x16x32_bf16 v[52:55], v[100:103], v[80:83], 0\n\t"
    "s_waitcnt lgkmcnt(3)\n\t"
    "v_mfma_f32_16x16x32_bf16 v[40:43], v[112:115], v[84:87], v[40:43]\n\t"
    "ds_read_b128 v[76:79], v3 offset:17472\n\t"
    "v_mfma_f32_16x16x32_bf16 v[48:51], v[104:107], v[84:87], v[48:51]\n\t"
    "ds_read_b128 v[92:95], v3 offset:21504\n\t"
    "s_waitcnt lgkmcnt(3)\n\t"
    "v_cmp_lt_u32 vcc, 0, v8\n\t"
    "v_mfma_f32_16x16x32_bf16 v[56:59], v[96:99], v[84:87], 0\n\t"
    "s_and_b32 s44, vcc_hi, 0xf\n\t"
    "s_cbranch_scc1 L_slow36\n\t"
    "L_cont36:\n\t"
    "ds_read_b64_tr_b16 v[80:81], v5 offset:56448\n\t"
    "v_mfma_f32_16x16x32_bf16 v[44:47], v[116:119], v[72:75], v[44:47]\n\t"
    "ds_read_b64_tr_b16 v[82:83], v5 offset:59520\n\t"
    "ds_read_b64_tr_b16 v[84:85], v5 offset:56832\n\t"
    "ds_read_b64_tr_b16 v[86:87], v5 offset:59904\n\t"
    "v_mfma_f32_16x16x32_bf16 v[52:55], v[108:111], v[72:75], v[52:55]\n\t"
    "ds_read_b64_tr_b16 v[132:133], v5 offset:57216\n\t"
    "ds_read_b64_tr_b16 v[134:135], v5 offset:60288\n\t"
    "ds_read_b64_tr_b16 v[128:129], v5 offset:57600\n\t"
    "ds_read_b32 v8, v7\n\t"
    "v_mfma_f32_16x16x32_bf16 v[60:63], v[100:103], v[72:75], 0\n\t"
    "ds_read_b64_tr_b16 v[130:131], v5 offset:60672\n\t"
    "v_mov_b32 v10, 5\n\t"
    "ds_write_b32 v9, v10\n\t"
    "s_waitcnt lgkmcnt(12)\n\t"
    "v_mfma_f32_16x16x32_bf16 v[48:51], v[112:115], v[88:91], v[48:51]\n\t"
    "ds_read_b128 v[136:139], v3 offset:22848\n\t"
    "v_mfma_f32_16x16x32_bf16 v[56:59], v[104:107], v[88:91], v[56:59]\n\t"
    "v_mfma_f32_16x16x32_bf16 v[64:67], v[96:99], v[88:91], 0\n\t"
    "s_waitcnt lgkmcnt(12)\n\t"
    "v_mfma_f32_16x16x32_bf16 v[52:55], v[116:119], v[76:79], v[52:55]\n\t"
    "s_waitcnt lgkmcnt(3)\n\t"
    "v_cmp_lt_u32 vcc, 1, v8\n\t"
    "v_mfma_f32_16x16x32_bf16 v[60:63], v[108:111], v[76:79], v[60:63]\n\t"
    "s_and_b32 s44, vcc_hi, 0xff000000\n\t"
    "s_cbranch_scc1 L_slow37\n\t"
    "L_cont37:\n\t"
    "ds_read_b32 v8, v7\n\t"
    "v_mfma_f32_16x16x32_bf16 v[68:71], v[100:103], v[76:79], 0\n\t"
    "v_mfma_f32_16x16x32_bf16 v[56:59], v[112:115], v[92:95], v[56:59]\n\t"
    "v_mfma_f32_16x16x32_bf16 v[64:67], v[104:107], v[92:95], v[64:67]\n\t"
    "ds_read_b128 v[148:151], v3 offset:26880\n\t"
    "v_mfma_f32_16x16x32_bf16 v[72:75], v[96:99], v[92:95], 0\n\t"
    "s_waitcnt lgkmcnt(2)\n\t"
    "v_mfma_f32_16x16x32_bf16 v[60:63], v[116:119], v[136:139], v[60:63]\n\t"
    "ds_read_b128 v[144:147], v3 offset:28224\n\t"
    "s_waitcnt lgkmcnt(2)\n\t"
    "v_cmp_lt_u32 vcc, 0, v8\n\t"
    "v_mfma_f32_16x16x32_bf16 v[68:71], v[108:111], v[136:139], v[68:71]\n\t"
    "s_and_b32 s44, vcc_hi, 0xf0\n\t"
    "s_cbranch_scc1 L_slow38\n\t"
    "L_cont38:\n\t"
    "ds_read_b64_tr_b16 v[88:89], v5 offset:0\n\t"
    "v_mfma_f32_16x16x32_bf16 v[76:79], v[100:103], v[136:139], 0\n\t"
    "ds_read_b64_tr_b16 v[90:91], v5 offset:3072\n\t"
    "ds_read_b64_tr_b16 v[92:93], v5 offset:384\n\t"
    "ds_read_b64_tr_b16 v[94:95], v5 offset:3456\n\t"
    "v_mfma_f32_16x16x32_bf16 v[80:83], v[12:15], v[80:83], 0\n\t"
    "ds_read_b64_tr_b16 v[136:137], v5 offset:768\n\t"
    "ds_read_b64_tr_b16 v[138:139], v5 offset:3840\n\t"
    "ds_read_b64_tr_b16 v[140:141], v5 offset:1152\n\t"
    "v_mfma_f32_16x16x32_bf16 v[84:87], v[12:15], v[84:87], 0\n\t"
    "ds_read_b64_tr_b16 v[142:143], v5 offset:4224\n\t"
    "v_mov_b32 v10, 4\n\t"
    "ds_write_b32 v9, v10\n\t"
    "v_mfma_f32_16x16x32_bf16 v[132:135], v[12:15], v[132:135], 0\n\t"
    "v_cvt_pk_bf16_f32 v120, v80, v81\n\t"
    "v_cvt_pk_bf16_f32 v124, v82, v83\n\t"
    "v_mfma_f32_16x16x32_bf16 v[128:131], v[12:15], v[128:131], 0\n\t"
    "s_nop 0\n\t"
    "v_cvt_pk_bf16_f32 v121, v84, v85\n\t"
    "v_cvt_pk_bf16_f32 v125, v86, v87\n\t"
    "s_waitcnt lgkmcnt(10)\n\t"
    "v_mfma_f32_16x16x32_bf16 v[64:67], v[112:115], v[148:151], v[64:67]\n\t"
    "v_cvt_pk_bf16_f32 v122, v132, v133\n\t"
    "v_cvt_pk_bf16_f32 v126, v134, v135\n\t"
    "v_mfma_f32_16x16x32_bf16 v[72:75], v[104:107], v[148:151], v[72:75]\n\t"
    "v_cvt_pk_bf16_f32 v123, v128, v129\n\t"
    "v_cvt_pk_bf16_f32 v127, v130, v131\n\t"
    "ds_read_b128 v[152:155], v3 offset:448\n\t"
    "v_mfma_f32_16x16x32_bf16 v[80:83], v[96:99], v[148:151], 0\n\t"
    "ds_read_b128 v[156:159], v3 offset:1792\n\t"
    "ds_read_b32 v8, v7\n\t"
    "s_waitcnt lgkmcnt(11)\n\t"
    "v_mfma_f32_16x16x32_bf16 v[68:71], v[116:119], v[144:147], v[68:71]\n\t"
    "ds_read_b128 v[160:163], v3 offset:32256\n\t"
    "v_mfma_f32_16x16x32_bf16 v[76:79], v[108:111], v[144:147], v[76:79]\n\t"
    "v_mfma_f32_16x16x32_bf16 v[84:87], v[100:103], v[144:147], 0\n\t"
    "s_waitcnt lgkmcnt(4)\n\t"
    "v_mfma_f32_16x16x32_bf16 v[88:91], v[12:15], v[88:91], 0\n\t"
    "ds_read_b128 v[164:167], v3 offset:33600\n\t"
    "v_mfma_f32_16x16x32_bf16 v[92:95], v[12:15], v[92:95], 0\n\t"
    "ds_read_b128 v[16:19], v3 offset:5824\n\t"
    "s_waitcnt lgkmcnt(3)\n\t"
    "v_cmp_lt_u32 vcc, 0, v8\n\t"
    "v_mfma_f32_16x16x32_bf16 v[136:139], v[12:15], v[136:139], 0\n\t"
    "s_nop 1\n\t"
    "v_cvt_pk_bf16_f32 v128, v88, v89\n\t"
    "v_cvt_pk_bf16_f32 v132, v90, v91\n\t"
    "s_and_b32 s44, vcc_hi, 0xf00\n\t"
    "s_cbranch_scc1 L_slow39\n\t"
    "L_cont39:\n\t"
    "v_mfma_f32_16x16x32_bf16 v[140:143], v[12:15], v[140:143], 0\n\t"
    "v_cvt_pk_bf16_f32 v129, v92, v93\n\t"
    "v_cvt_pk_bf16_f32 v133, v94, v95\n\t"
    "ds_read_b64_tr_b16 v[148:149], v5 offset:18816\n\t"
    "v_mfma_f32_16x16x32_bf16 v[40:43], v[120:123], v[152:155], v[40:43]\n\t"
    "v_cvt_pk_bf16_f32 v130, v136, v137\n\t"
    "v_cvt_pk_bf16_f32 v134, v138, v139\n\t"
    "ds_read_b64_tr_b16 v[150:151], v5 offset:21888\n\t"
    "v_mfma_f32_16x16x32_bf16 v[44:47], v[124:127], v[156:159], v[44:47]\n\t"
    "v_cvt_pk_bf16_f32 v131, v140, v141\n\t"
    "v_cvt_pk_bf16_f32 v135, v142, v143\n\t"
    "ds_read_b64_tr_b16 v[144:145], v5 offset:19200\n\t"
    "ds_read_b128 v[20:23], v3 offset:7168\n\t"
    "s_waitcnt lgkmcnt(6)\n\t"
    "v_mfma_f32_16x16x32_bf16 v[72:75], v[112:115], v[160:163], v[72:75]\n\t"
    "ds_read_b64_tr_b16 v[146:147], v5 offset:22272\n\t"
    "ds_read_b64_tr_b16 v[152:153], v5 offset:19584\n\t"
    "ds_read_b64_tr_b16 v[154:155], v5 offset:22656\n\t"
    "v_mfma_f32_16x16x32_bf16 v[80:83], v[104:107], v[160:163], v[80:83]\n\t"
    "ds_read_b64_tr_b16 v[156:157], v5 offset:19968\n\t"
    "ds_read_b64_tr_b16 v[158:159], v5 offset:23040\n\t"
    "v_mfma_f32_16x16x32_bf16 v[88:91], v[96:99], v[160:163], 0\n\t"
    "s_waitcnt lgkmcnt(10)\n\t"
    "v_mfma_f32_16x16x32_bf16 v[76:79], v[116:119], v[164:167], v[76:79]\n\t"
    "v_mfma_f32_16x16x32_bf16 v[84:87], v[108:111], v[164:167], v[84:87]\n\t"
    "v_mfma_f32_16x16x32_bf16 v[92:95], v[100:103], v[164:167], 0\n\t"
    "ds_read_b128 v[160:163], v3 offset:37632\n\t"
    "s_waitcnt lgkmcnt(9)\n\t"
    "v_mfma_f32_16x16x32_bf16 v[40:43], v[128:131], v[16:19], v[40:43]\n\t"
    "v_mfma_f32_16x16x32_bf16 v[48:51], v[120:123], v[16:19], v[48:51]\n\t"
    "ds_read_b128 v[164:167], v3 offset:38976\n\t"
    "s_waitcnt lgkmcnt(7)\n\t"
    "v_mfma_f32_16x16x32_bf16 v[44:47], v[132:135], v[20:23], v[44:47]\n\t"
    "v_mfma_f32_16x16x32_bf16 v[52:55], v[124:127], v[20:23], v[52:55]\n\t"
    "ds_read_b128 v[100:103], v3 offset:11200\n\t"
    "v_mfma_f32_16x16x32_bf16 v[148:151], v[12:15], v[148:151], 0\n\t"
    "ds_read_b32 v8, v7\n\t"
    "s_waitcnt lgkmcnt(4)\n\t"
    "v_mfma_f32_16x16x32_bf16 v[144:147], v[12:15], v[144:147], 0\n\t"
    "ds_read_b128 v[96:99], v3 offset:12544\n\t"
    "v_mfma_f32_16x16x32_bf16 v[152:155], v[12:15], v[152:155], 0\n\t"
    "s_nop 2\n\t"
    "v_cvt_pk_bf16_f32 v136, v148, v149\n\t"
    "v_cvt_pk_bf16_f32 v140, v150, v151\n\t"
    "v_mfma_f32_16x16x32_bf16 v[156:159], v[12:15], v[156:159], 0\n\t"
    "v_cvt_pk_bf16_f32 v137, v144, v145\n\t"
    "v_cvt_pk_bf16_f32 v141, v146, v147\n\t"
    "s_setprio 2\n\t"
    "s_waitcnt lgkmcnt(4)\n\t"
    "v_mfma_f32_16x16x32_bf16 v[80:83], v[112:115], v[160:163], v[80:83]\n\t"
    "v_cvt_pk_bf16_f32 v138, v152, v153\n\t"
    "v_cvt_pk_bf16_f32 v142, v154, v155\n\t"
    "v_mfma_f32_16x16x32_bf16 v[88:91], v[104:107], v[160:163], v[88:91]\n\t"
    "v_cvt_pk_bf16_f32 v139, v156, v157\n\t"
    "v_cvt_pk_bf16_f32 v143, v158, v159\n\t"
    "ds_read_b128 v[148:151], v3 offset:16576\n\t"
    "s_waitcnt lgkmcnt(2)\n\t"
    "v_cmp_lt_u32 vcc, 0, v8\n\t"
    "v_mfma_f32_16x16x32_bf16 v[84:87], v[116:119], v[164:167], v[84:87]\n\t"
    "s_and_b32 s44, vcc_hi, 0xf000\n\t"
    "s_cbranch_scc1 L_slow40\n\t"
    "L_cont40:\n\t"
    "ds_read_b64_tr_b16 v[156:157], v5 offset:37632\n\t"
    "v_mfma_f32_16x16x32_bf16 v[92:95], v[108:111], v[164:167], v[92:95]\n\t"
    "ds_read_b64_tr_b16 v[158:159], v5 offset:40704\n\t"
    "ds_read_b64_tr_b16 v[152:153], v5 offset:38016\n\t"
    "ds_read_b64_tr_b16 v[154:155], v5 offset:41088\n\t"
    "v_mfma_f32_16x16x32_bf16 v[40:43], v[136:139], v[100:103], v[40:43]\n\t"
    "ds_read_b64_tr_b16 v[160:161], v5 offset:38400\n\t"
    "ds_read_b64_tr_b16 v[162:163], v5 offset:41472\n\t"
    "ds_read_b64_tr_b16 v[164:165], v5 offset:38784\n\t"
    "ds_read_b128 v[104:107], v3 offset:17920\n\t"
    "v_mfma_f32_16x16x32_bf16 v[48:51], v[128:131], v[100:103], v[48:51]\n\t"
    "ds_read_b64_tr_b16 v[166:167], v5 offset:41856\n\t"
    "v_mfma_f32_16x16x32_bf16 v[56:59], v[120:123], v[100:103], v[56:59]\n\t"
    "s_waitcnt lgkmcnt(10)\n\t"
    "v_mfma_f32_16x16x32_bf16 v[44:47], v[140:143], v[96:99], v[44:47]\n\t"
    "v_mfma_f32_16x16x32_bf16 v[52:55], v[132:135], v[96:99], v[52:55]\n\t"
    "v_mfma_f32_16x16x32_bf16 v[60:63], v[124:127], v[96:99], v[60:63]\n\t"
    "s_waitcnt lgkmcnt(9)\n\t"
    "v_mfma_f32_16x16x32_bf16 v[48:51], v[136:139], v[148:151], v[48:51]\n\t"
    "ds_read_b32 v8, v7\n\t"
    "v_mfma_f32_16x16x32_bf16 v[56:59], v[128:131], v[148:151], v[56:59]\n\t"
    "ds_read_b128 v[20:23], v3 offset:21952\n\t"
    "v_mfma_f32_16x16x32_bf16 v[64:67], v[120:123], v[148:151], v[64:67]\n\t"
    "s_waitcnt lgkmcnt(3)\n\t"
    "v_mfma_f32_16x16x32_bf16 v[52:55], v[140:143], v[104:107], v[52:55]\n\t"
    "v_mfma_f32_16x16x32_bf16 v[60:63], v[132:135], v[104:107], v[60:63]\n\t"
    "ds_read_b128 v[16:19], v3 offset:23296\n\t"
    "v_mfma_f32_16x16x32_bf16 v[68:71], v[124:127], v[104:107], v[68:71]\n\t"
    "ds_read_b128 v[24:27], v3 offset:896\n\t"
    "s_waitcnt lgkmcnt(3)\n\t"
    "v_cmp_lt_u32 vcc, 0, v8\n\t"
    "v_mfma_f32_16x16x32_bf16 v[156:159], v[12:15], v[156:159], 0\n\t"
    "s_and_b32 s44, vcc_hi, 0xf0000\n\t"
    "s_cbranch_scc1 L_slow41\n\t"
    "L_cont41:\n\t"
    "ds_read_b64_tr_b16 v[108:109], v5 offset:56448\n\t"
    "v_mfma_f32_16x16x32_bf16 v[152:155], v[12:15], v[152:155], 0\n\t"
    "ds_read_b64_tr_b16 v[110:111], v5 offset:59520\n\t"
    "ds_read_b64_tr_b16 v[104:105], v5 offset:56832\n\t"
    "ds_read_b64_tr_b16 v[106:107], v5 offset:59904\n\t"
    "v_mfma_f32_16x16x32_bf16 v[160:163], v[12:15], v[160:163], 0\n\t"
    "v_cvt_pk_bf16_f32 v144, v156, v157\n\t"
    "v_cvt_pk_bf16_f32 v148, v158, v159\n\t"
    "ds_read_b64_tr_b16 v[100:101], v5 offset:57216\n\t"
    "ds_read_b128 v[28:31], v3 offset:2240\n\t"
    "v_mfma_f32_16x16x32_bf16 v[164:167], v[12:15], v[164:167], 0\n\t"
    "v_cvt_pk_bf16_f32 v145, v152, v153\n\t"
    "v_cvt_pk_bf16_f32 v149, v154, v155\n\t"
    "ds_read_b64_tr_b16 v[102:103], v5 offset:60288\n\t"
    "s_waitcnt lgkmcnt(9)\n\t"
    "v_mfma_f32_16x16x32_bf16 v[56:59], v[136:139], v[20:23], v[56:59]\n\t"
    "v_cvt_pk_bf16_f32 v146, v160, v161\n\t"
    "v_cvt_pk_bf16_f32 v150, v162, v163\n\t"
    "ds_read_b64_tr_b16 v[96:97], v5 offset:57600\n\t"
    "v_mfma_f32_16x16x32_bf16 v[64:67], v[128:131], v[20:23], v[64:67]\n\t"
    "v_cvt_pk_bf16_f32 v147, v164, v165\n\t"
    "v_cvt_pk_bf16_f32 v151, v166, v167\n\t"
    "ds_read_b64_tr_b16 v[98:99], v5 offset:60672\n\t"
    "v_mfma_f32_16x16x32_bf16 v[72:75], v[120:123], v[20:23], v[72:75]\n\t"
    "v_mov_b32 v10, 3\n\t"
    "ds_write_b32 v9, v10\n\t"
    "s_waitcnt lgkmcnt(11)\n\t"
    "v_mfma_f32_16x16x32_bf16 v[60:63], v[140:143], v[16:19], v[60:63]\n\t"
    "ds_read_b128 v[160:163], v3 offset:27328\n\t"
    "v_mfma_f32_16x16x32_bf16 v[68:71], v[132:135], v[16:19], v[68:71]\n\t"
    "ds_read_b32 v8, v7\n\t"
    "v_mfma_f32_16x16x32_bf16 v[76:79], v[124:127], v[16:19], v[76:79]\n\t"
    "s_waitcnt lgkmcnt(8)\n\t"
    "v_mfma_f32_16x16x32_bf16 v[40:43], v[144:147], v[24:27], v[40:43]\n\t"
    "ds_read_b128 v[164:167], v3 offset:28672\n\t"
    "s_waitcnt lgkmcnt(7)\n\t"
    "v_mfma_f32_16x16x32_bf16 v[44:47], v[148:151], v[28:31], v[44:47]\n\t"
    "v_mfma_f32_16x16x32_bf16 v[108:111], v[12:15], v[108:111], 0\n\t"
    "ds_read_b128 v[28:31], v3 offset:6272\n\t"
    "v_mfma_f32_16x16x32_bf16 v[104:107], v[12:15], v[104:107], 0\n\t"
    "ds_read_b128 v[24:27], v3 offset:7616\n\t"
    "s_waitcnt lgkmcnt(3)\n\t"
    "v_cmp_lt_u32 vcc, 0, v8\n\t"
    "v_mfma_f32_16x16x32_bf16 v[100:103], v[12:15], v[100:103], 0\n\t"
    "s_nop 1\n\t"
    "v_cvt_pk_bf16_f32 v152, v108, v109\n\t"
    "v_cvt_pk_bf16_f32 v156, v110, v111\n\t"
    "s_and_b32 s44, vcc_hi, 0xf00000\n\t"
    "s_cbranch_scc1 L_slow42\n\t"
    "L_cont42:\n\t"
    "v_mfma_f32_16x16x32_bf16 v[96:99], v[12:15], v[96:99], 0\n\t"
    "v_cvt_pk_bf16_f32 v153, v104, v105\n\t"
    "v_cvt_pk_bf16_f32 v157, v106, v107\n\t"
    "ds_read_b64_tr_b16 v[108:109], v5 offset:0\n\t"
    "v_mfma_f32_16x16x32_bf16 v[64:67], v[136:139], v[160:163], v[64:67]\n\t"
    "v_cvt_pk_bf16_f32 v154, v100, v101\n\t"
    "v_cvt_pk_bf16_f32 v158, v102, v103\n\t"
    "ds_read_b64_tr_b16 v[110:111], v5 offset:3072\n\t"
    "v_mfma_f32_16x16x32_bf16 v[72:75], v[128:131], v[160:163], v[72:75]\n\t"
    "v_cvt_pk_bf16_f32 v155, v96, v97\n\t"
    "v_cvt_pk_bf16_f32 v159, v98, v99\n\t"
    "ds_read_b64_tr_b16 v[100:101], v5 offset:384\n\t"
    "v_mfma_f32_16x16x32_bf16 v[80:83], v[120:123], v[160:163], v[80:83]\n\t"
    "ds_read_b64_tr_b16 v[102:103], v5 offset:3456\n\t"
    "ds_read_b64_tr_b16 v[96:97], v5 offset:768\n\t"
    "ds_read_b64_tr_b16 v[98:99], v5 offset:3840\n\t"
    "s_waitcnt lgkmcnt(8)\n\t"
    "v_mfma_f32_16x16x32_bf16 v[68:71], v[140:143], v[164:167], v[68:71]\n\t"
    "ds_read_b64_tr_b16 v[104:105], v5 offset:1152\n\t"
    "ds_read_b64_tr_b16 v[106:107], v5 offset:4224\n\t"
    "v_mfma_f32_16x16x32_bf16 v[76:79], v[132:135], v[164:167], v[76:79]\n\t"
    "v_mfma_f32_16x16x32_bf16 v[84:87], v[124:127], v[164:167], v[84:87]\n\t"
    "ds_read_b128 v[16:19], v3 offset:32704\n\t"
    "s_waitcnt lgkmcnt(9)\n\t"
    "v_mfma_f32_16x16x32_bf16 v[40:43], v[152:155], v[28:31], v[40:43]\n\t"
    "v_mfma_f32_16x16x32_bf16 v[48:51], v[144:147], v[28:31], v[48:51]\n\t"
    "v_mfma_f32_16x16x32_bf16 v[44:47], v[156:159], v[24:27], v[44:47]\n\t"
    "ds_read_b128 v[20:23], v3 offset:34048\n\t"
    "v_mfma_f32_16x16x32_bf16 v[52:55], v[148:151], v[24:27], v[52:55]\n\t"
    "s_waitcnt lgkmcnt(7)\n\t"
    "v_mfma_f32_16x16x32_bf16 v[108:111], v[12:15], v[108:111], 0\n\t"
    "s_waitcnt lgkmcnt(4)\n\t"
    "v_mfma_f32_16x16x32_bf16 v[100:103], v[12:15], v[100:103], 0\n\t"
    "ds_read_b128 v[24:27], v3 offset:11648\n\t"
    "v_mfma_f32_16x16x32_bf16 v[96:99], v[12:15], v[96:99], 0\n\t"
    "s_nop 3\n\t"
    "v_cvt_pk_bf16_f32 v160, v108, v109\n\t"
    "v_cvt_pk_bf16_f32 v164, v110, v111\n\t"
    "s_waitcnt lgkmcnt(3)\n\t"
    "v_mfma_f32_16x16x32_bf16 v[104:107], v[12:15], v[104:107], 0\n\t"
    "v_cvt_pk_bf16_f32 v161, v100, v101\n\t"
    "v_cvt_pk_bf16_f32 v165, v102, v103\n\t"
    "s_waitcnt lgkmcnt(2)\n\t"
    "v_mfma_f32_16x16x32_bf16 v[72:75], v[136:139], v[16:19], v[72:75]\n\t"
    "v_cvt_pk_bf16_f32 v162, v96, v97\n\t"
    "v_cvt_pk_bf16_f32 v166, v98, v99\n\t"
    "ds_read_b128 v[28:31], v3 offset:12992\n\t"
    "v_mfma_f32_16x16x32_bf16 v[80:83], v[128:131], v[16:19], v[80:83]\n\t"
    "v_cvt_pk_bf16_f32 v163, v104, v105\n\t"
    "v_cvt_pk_bf16_f32 v167, v106, v107\n\t"
    "v_mfma_f32_16x16x32_bf16 v[88:91], v[120:123], v[16:19], v[88:91]\n\t"
    "s_waitcnt lgkmcnt(2)\n\t"
    "v_mfma_f32_16x16x32_bf16 v[76:79], v[140:143], v[20:23], v[76:79]\n\t"
    "ds_read_b128 v[96:99], v3 offset:17024\n\t"
    "v_mfma_f32_16x16x32_bf16 v[84:87], v[132:135], v[20:23], v[84:87]\n\t"
    "v_mfma_f32_16x16x32_bf16 v[92:95], v[124:127], v[20:23], v[92:95]\n\t"
    "ds_read_b32 v8, v7\n\t"
    "s_waitcnt lgkmcnt(3)\n\t"
    "v_mfma_f32_16x16x32_bf16 v[40:43], v[160:163], v[24:27], v[40:43]\n\t"
    "ds_read_b128 v[16:19], v3 offset:18368\n\t"
    "v_mfma_f32_16x16x32_bf16 v[48:51], v[152:155], v[24:27], v[48:51]\n\t"
    "s_setprio 1\n\t"
    "v_mfma_f32_16x16x32_bf16 v[56:59], v[144:147], v[24:27], v[56:59]\n\t"
    "s_waitcnt lgkmcnt(3)\n\t"
    "v_mfma_f32_16x16x32_bf16 v[44:47], v[164:167], v[28:31], v[44:47]\n\t"
    "ds_read_b128 v[20:23], v3 offset:22400\n\t"
    "v_mfma_f32_16x16x32_bf16 v[52:55], v[156:159], v[28:31], v[52:55]\n\t"
    "ds_read_b128 v[24:27], v3 offset:23744\n\t"
    "s_waitcnt lgkmcnt(3)\n\t"
    "v_cmp_lt_u32 vcc, 3, v8\n\t"
    "v_mfma_f32_16x16x32_bf16 v[60:63], v[148:151], v[28:31], v[60:63]\n\t"
    "s_and_b32 s44, vcc_lo, 0xfff\n\t"
    "s_cbranch_scc1 L_slow43\n\t"
    "L_cont43:\n\t"
    "v_mfma_f32_16x16x32_bf16 v[48:51], v[160:163], v[96:99], v[48:51]\n\t"
    "v_cvt_pk_bf16_f32 v40, v40, v41\n\t"
    "v_cvt_pk_bf16_f32 v41, v42, v43\n\t"
    "v_mfma_f32_16x16x32_bf16 v[56:59], v[152:155], v[96:99], v[56:59]\n\t"
    "v_cvt_pk_bf16_f32 v42, v44, v45\n\t"
    "v_cvt_pk_bf16_f32 v43, v46, v47\n\t"
    "v_mfma_f32_16x16x32_bf16 v[64:67], v[144:147], v[96:99], v[64:67]\n\t"
    "ds_write_b128 v11, v[40:43] offset:18816\n\t"
    "s_waitcnt lgkmcnt(3)\n\t"
    "v_mfma_f32_16x16x32_bf16 v[52:55], v[164:167], v[16:19], v[52:55]\n\t"
    "v_mov_b32 v10, 2\n\t"
    "ds_write_b32 v9, v10\n\t"
    "ds_read_b128 v[28:31], v3 offset:27776\n\t"
    "v_mfma_f32_16x16x32_bf16 v[60:63], v[156:159], v[16:19], v[60:63]\n\t"
    "ds_read_b128 v[40:43], v3 offset:29120\n\t"
    "v_mfma_f32_16x16x32_bf16 v[68:71], v[148:151], v[16:19], v[68:71]\n\t"
    "v_cvt_pk_bf16_f32 v48, v48, v49\n\t"
    "v_cvt_pk_bf16_f32 v49, v50, v51\n\t"
    "s_waitcnt lgkmcnt(5)\n\t"
    "v_mfma_f32_16x16x32_bf16 v[56:59], v[160:163], v[20:23], v[56:59]\n\t"
    "v_cvt_pk_bf16_f32 v50, v52, v53\n\t"
    "v_cvt_pk_bf16_f32 v51, v54, v55\n\t"
    "v_mfma_f32_16x16x32_bf16 v[64:67], v[152:155], v[20:23], v[64:67]\n\t"
    "ds_write_b128 v11, v[48:51] offset:21504\n\t"
    "v_mfma_f32_16x16x32_bf16 v[72:75], v[144:147], v[20:23], v[72:75]\n\t"
    "v_mov_b32 v10, 1\n\t"
    "ds_write_b32 v9, v10\n\t"
    "s_waitcnt lgkmcnt(6)\n\t"
    "v_mfma_f32_16x16x32_bf16 v[60:63], v[164:167], v[24:27], v[60:63]\n\t"
    "ds_read_b128 v[52:55], v3 offset:33152\n\t"
    "v_mfma_f32_16x16x32_bf16 v[68:71], v[156:159], v[24:27], v[68:71]\n\t"
    "ds_read_b128 v[20:23], v3 offset:34496\n\t"
    "v_mfma_f32_16x16x32_bf16 v[76:79], v[148:151], v[24:27], v[76:79]\n\t"
    "v_cvt_pk_bf16_f32 v56, v56, v57\n\t"
    "v_cvt_pk_bf16_f32 v57, v58, v59\n\t"
    "s_waitcnt lgkmcnt(5)\n\t"
    "v_mfma_f32_16x16x32_bf16 v[64:67], v[160:163], v[28:31], v[64:67]\n\t"
    "v_cvt_pk_bf16_f32 v58, v60, v61\n\t"
    "v_cvt_pk_bf16_f32 v59, v62, v63\n\t"
    "v_mfma_f32_16x16x32_bf16 v[72:75], v[152:155], v[28:31], v[72:75]\n\t"
    "ds_write_b128 v11, v[56:59] offset:24192\n\t"
    "v_mfma_f32_16x16x32_bf16 v[80:83], v[144:147], v[28:31], v[80:83]\n\t"
    "s_waitcnt lgkmcnt(5)\n\t"
    "v_mfma_f32_16x16x32_bf16 v[68:71], v[164:167], v[40:43], v[68:71]\n\t"
    "ds_read_b128 v[60:63], v3 offset:38080\n\t"
    "v_mfma_f32_16x16x32_bf16 v[76:79], v[156:159], v[40:43], v[76:79]\n\t"
    "ds_read_b128 v[28:31], v3 offset:39424\n\t"
    "v_mfma_f32_16x16x32_bf16 v[84:87], v[148:151], v[40:43], v[84:87]\n\t"
    "v_cvt_pk_bf16_f32 v64, v64, v65\n\t"
    "v_cvt_pk_bf16_f32 v65, v66, v67\n\t"
    "s_waitcnt lgkmcnt(4)\n\t"
    "v_mfma_f32_16x16x32_bf16 v[72:75], v[160:163], v[52:55], v[72:75]\n\t"
    "v_cvt_pk_bf16_f32 v66, v68, v69\n\t"
    "v_cvt_pk_bf16_f32 v67, v70, v71\n\t"
    "v_mfma_f32_16x16x32_bf16 v[80:83], v[152:155], v[52:55], v[80:83]\n\t"
    "ds_write_b128 v11, v[64:67] offset:26880\n\t"
    "ds_read_b128 v[40:43], v3 offset:38528\n\t"
    "v_mfma_f32_16x16x32_bf16 v[88:91], v[144:147], v[52:55], v[88:91]\n\t"
    "ds_read_b128 v[68:71], v3 offset:39872\n\t"
    "s_waitcnt lgkmcnt(6)\n\t"
    "v_mfma_f32_16x16x32_bf16 v[76:79], v[164:167], v[20:23], v[76:79]\n\t"
    "ds_read_b128 v[52:55], v3 offset:43008\n\t"
    "v_mfma_f32_16x16x32_bf16 v[84:87], v[156:159], v[20:23], v[84:87]\n\t"
    "ds_read_b128 v[64:67], v3 offset:44352\n\t"
    "v_mfma_f32_16x16x32_bf16 v[92:95], v[148:151], v[20:23], v[92:95]\n\t"
    "v_cvt_pk_bf16_f32 v72, v72, v73\n\t"
    "v_cvt_pk_bf16_f32 v73, v74, v75\n\t"
    "s_waitcnt lgkmcnt(6)\n\t"
    "v_mfma_f32_16x16x32_bf16 v[80:83], v[136:139], v[60:63], v[80:83]\n\t"
    "v_cvt_pk_bf16_f32 v74, v76, v77\n\t"
    "v_cvt_pk_bf16_f32 v75, v78, v79\n\t"
    "v_mfma_f32_16x16x32_bf16 v[88:91], v[128:131], v[60:63], v[88:91]\n\t"
    "ds_write_b128 v11, v[72:75] offset:29568\n\t"
    "ds_read_b128 v[20:23], v3 offset:43456\n\t"
    "s_waitcnt lgkmcnt(7)\n\t"
    "v_mfma_f32_16x16x32_bf16 v[84:87], v[140:143], v[28:31], v[84:87]\n\t"
    "ds_read_b128 v[148:151], v3 offset:44800\n\t"
    "v_mfma_f32_16x16x32_bf16 v[92:95], v[132:135], v[28:31], v[92:95]\n\t"
    "ds_read_b128 v[60:63], v3 offset:43904\n\t"
    "s_waitcnt lgkmcnt(7)\n\t"
    "v_mfma_f32_16x16x32_bf16 v[80:83], v[160:163], v[40:43], v[80:83]\n\t"
    "ds_read_b128 v[72:75], v3 offset:45248\n\t"
    "v_mfma_f32_16x16x32_bf16 v[88:91], v[152:155], v[40:43], v[88:91]\n\t"
    "s_waitcnt lgkmcnt(6)\n\t"
    "v_mfma_f32_16x16x32_bf16 v[84:87], v[164:167], v[68:71], v[84:87]\n\t"
    "v_mfma_f32_16x16x32_bf16 v[92:95], v[156:159], v[68:71], v[92:95]\n\t"
    "v_mfma_f32_16x16x32_bf16 v[88:91], v[112:115], v[52:55], v[88:91]\n\t"
    "s_nop 1\n\t"
    "v_cvt_pk_bf16_f32 v80, v80, v81\n\t"
    "v_cvt_pk_bf16_f32 v81, v82, v83\n\t"
    "s_waitcnt lgkmcnt(5)\n\t"
    "v_mfma_f32_16x16x32_bf16 v[92:95], v[116:119], v[64:67], v[92:95]\n\t"
    "v_cvt_pk_bf16_f32 v82, v84, v85\n\t"
    "v_cvt_pk_bf16_f32 v83, v86, v87\n\t"
    "s_waitcnt lgkmcnt(3)\n\t"
    "v_mfma_f32_16x16x32_bf16 v[88:91], v[136:139], v[20:23], v[88:91]\n\t"
    "ds_write_b128 v11, v[80:83] offset:32256\n\t"
    "s_waitcnt lgkmcnt(3)\n\t"
    "v_mfma_f32_16x16x32_bf16 v[92:95], v[140:143], v[148:151], v[92:95]\n\t"
    "s_waitcnt lgkmcnt(2)\n\t"
    "v_mfma_f32_16x16x32_bf16 v[88:91], v[160:163], v[60:63], v[88:91]\n\t"
    "s_waitcnt lgkmcnt(1)\n\t"
    "v_mfma_f32_16x16x32_bf16 v[92:95], v[164:167], v[72:75], v[92:95]\n\t"
    "s_nop 5\n\t"
    "v_cvt_pk_bf16_f32 v88, v88, v89\n\t"
    "v_cvt_pk_bf16_f32 v89, v90, v91\n\t"
    "v_cvt_pk_bf16_f32 v90, v92, v93\n\t"
    "v_cvt_pk_bf16_f32 v91, v94, v95\n\t"
    "ds_write_b128 v11, v[88:91] offset:34944\n\t"
    "v_mov_b32 v10, 0\n\t"
    "ds_write_b32 v9, v10\n\t"
    "L_finF:\n\t"
    "s_branch L_end\n\t"
    "L_G:\n\t"
    "s_waitcnt lgkmcnt(0)\n\t"
    "s_barrier\n\t"
    "s_setprio 2\n\t"
    "L_poll44:\n\t"
    "ds_read_b32 v8, v7\n\t"
    "s_waitcnt lgkmcnt(0)\n\t"
    "v_cmp_lt_u32 vcc, 0, v8\n\t"
    "s_and_b32 s44, vcc_lo, 0xf000\n\t"
    "s_cbranch_scc0 L_polld44\n\t"
    "s_sub_u32 s43, s43, 1\n\t"
    "s_cbranch_scc1 L_end\n\t"
    "s_branch L_poll44\n\t"
    "L_polld44:\n\t"
    "ds_read_b64_tr_b16 v[16:17], v123 offset:0\n\t"
    "ds_read_b64_tr_b16 v[20:21], v123 offset:1536\n\t"
    "ds_read_b64_tr_b16 v[18:19], v123 offset:768\n\t"
    "ds_read_b64_tr_b16 v[22:23], v123 offset:2304\n\t"
    "ds_read_b64_tr_b16 v[24:25], v123 offset:6144\n\t"
    "ds_read_b64_tr_b16 v[28:29], v123 offset:7680\n\t"
    "ds_read_b64_tr_b16 v[26:27], v123 offset:6912\n\t"
    "ds_read_b64_tr_b16 v[30:31], v123 offset:8448\n\t"
    "ds_read_b64_tr_b16 v[32:33], v123 offset:12288\n\t"
    "ds_read_b64_tr_b16 v[36:37], v123 offset:13824\n\t"
    "ds_read_b64_tr_b16 v[34:35], v123 offset:13056\n\t"
    "ds_read_b64_tr_b16 v[38:39], v123 offset:14592\n\t"
    "ds_read_b64_tr_b16 v[40:41], v123 offset:64\n\t"
    "ds_read_b64_tr_b16 v[44:45], v123 offset:1600\n\t"
    "ds_read_b64_tr_b16 v[42:43], v123 offset:832\n\t"
    "ds_read_b64_tr_b16 v[46:47], v123 offset:2368\n\t"
    "ds_read_b64_tr_b16 v[48:49], v123 offset:6208\n\t"
    "ds_read_b64_tr_b16 v[52:53], v123 offset:7744\n\t"
    "ds_read_b64_tr_b16 v[50:51], v123 offset:6976\n\t"
    "ds_read_b64_tr_b16 v[54:55], v123 offset:8512\n\t"
    "ds_read_b64_tr_b16 v[56:57], v123 offset:12352\n\t"
    "ds_read_b64_tr_b16 v[60:61], v123 offset:13888\n\t"
    "ds_read_b64_tr_b16 v[58:59], v123 offset:13120\n\t"
    "ds_read_b64_tr_b16 v[62:63], v123 offset:14656\n\t"
    "ds_read_b64_tr_b16 v[64:65], v123 offset:128\n\t"
    "ds_read_b64_tr_b16 v[68:69], v123 offset:1664\n\t"
    "ds_read_b64_tr_b16 v[66:67], v123 offset:896\n\t"
    "ds_read_b64_tr_b16 v[70:71], v123 offset:2432\n\t"
    "ds_read_b64_tr_b16 v[72:73], v123 offset:6272\n\t"
    "ds_read_b64_tr_b16 v[76:77], v123 offset:7808\n\t"
    "ds_read_b64_tr_b16 v[74:75], v123 offset:7040\n\t"
    "ds_read_b64_tr_b16 v[78:79], v123 offset:8576\n\t"
    "ds_read_b64_tr_b16 v[80:81], v123 offset:12416\n\t"
    "ds_read_b64_tr_b16 v[84:85], v123 offset:13952\n\t"
    "ds_read_b64_tr_b16 v[82:83], v123 offset:13184\n\t"
    "ds_read_b64_tr_b16 v[86:87], v123 offset:14720\n\t"
    "s_waitcnt lgkmcnt(15)\n\t"
    "v_permlane16_swap_b32 v16, v20\n\t"
    "v_permlane16_swap_b32 v17, v21\n\t"
    "v_permlane16_swap_b32 v18, v22\n\t"
    "v_permlane16_swap_b32 v19, v23\n\t"
    "v_permlane16_swap_b32 v24, v28\n\t"
    "v_permlane16_swap_b32 v25, v29\n\t"
    "v_permlane16_swap_b32 v26, v30\n\t"
    "v_permlane16_swap_b32 v27, v31\n\t"
    "v_permlane16_swap_b32 v32, v36\n\t"
    "v_permlane16_swap_b32 v33, v37\n\t"
    "v_permlane16_swap_b32 v34, v38\n\t"
    "v_permlane16_swap_b32 v35, v39\n\t"
    "v_permlane16_swap_b32 v40, v44\n\t"
    "v_permlane16_swap_b32 v41, v45\n\t"
    "v_permlane16_swap_b32 v42, v46\n\t"
    "v_permlane16_swap_b32 v43, v47\n\t"
    "v_permlane16_swap_b32 v48, v52\n\t"
    "v_permlane16_swap_b32 v49, v53\n\t"
    "v_permlane16_swap_b32 v50, v54\n\t"
    "v_permlane16_swap_b32 v51, v55\n\t"
    "s_waitcnt lgkmcnt(14)\n\t"
    "v_permlane16_swap_b32 v56, v60\n\t"
    "v_permlane16_swap_b32 v57, v61\n\t"
    "s_waitcnt lgkmcnt(12)\n\t"
    "v_permlane16_swap_b32 v58, v62\n\t"
    "v_permlane16_swap_b32 v59, v63\n\t"
    "s_waitcnt lgkmcnt(10)\n\t"
    "v_permlane16_swap_b32 v64, v68\n\t"
    "v_permlane16_swap_b32 v65, v69\n\t"
    "s_waitcnt lgkmcnt(8)\n\t"
    "v_permlane16_swap_b32 v66, v70\n\t"
    "v_permlane16_swap_b32 v67, v71\n\t"
    "s_waitcnt lgkmcnt(6)\n\t"
    "v_permlane16_swap_b32 v72, v76\n\t"
    "v_permlane16_swap_b32 v73, v77\n\t"
    "s_waitcnt lgkmcnt(4)\n\t"
    "v_permlane16_swap_b32 v74, v78\n\t"
    "v_permlane16_swap_b32 v75, v79\n\t"
    "s_waitcnt lgkmcnt(2)\n\t"
    "v_permlane16_swap_b32 v80, v84\n\t"
    "v_permlane16_swap_b32 v81, v85\n\t"
    "s_waitcnt lgkmcnt(0)\n\t"
    "v_permlane16_swap_b32 v82, v86\n\t"
    "v_permlane16_swap_b32 v83, v87\n\t"
    "ds_read_b32 v8, v7\n\t"
    "ds_read_b128 v[156:159], v124 offset:0\n\t"
    "ds_read_b128 v[164:167], v125 offset:0\n\t"
    "ds_read_b128 v[88:91], v124 offset:64\n\t"
    "ds_read_b128 v[92:95], v125 offset:64\n\t"
    "ds_read_b128 v[112:115], v124 offset:128\n\t"
    "ds_read_b128 v[116:119], v125 offset:128\n\t"
    "s_waitcnt lgkmcnt(6)\n\t"
    "v_cmp_lt_u32 vcc, 0, v8\n\t"
    "s_and_b32 s44, vcc_lo, 0xf0000\n\t"
    "s_cbranch_scc1 L_poll45\n\t"
    "L_polld45:\n\t"
    "v_mov_b32 v10, 9\n\t"
    "ds_write_b32 v9, v10\n\t"
    "s_waitcnt lgkmcnt(6)\n\t"
    "v_lshlrev_b32 v152, 16, v156\n\t"
    "v_and_b32 v153, s45, v156\n\t"
    "v_lshlrev_b32 v154, 16, v157\n\t"
    "v_and_b32 v155, s45, v157\n\t"
    "v_lshlrev_b32 v156, 16, v158\n\t"
    "v_and_b32 v157, s45, v158\n\t"
    "v_lshlrev_b32 v158, 16, v159\n\t"
    "v_and_b32 v159, s45, v159\n\t"
    "s_waitcnt lgkmcnt(5)\n\t"
    "v_lshlrev_b32 v160, 16, v164\n\t"
    "v_and_b32 v161, s45, v164\n\t"
    "v_lshlrev_b32 v162, 16, v165\n\t"
    "v_and_b32 v163, s45, v165\n\t"
    "v_lshlrev_b32 v164, 16, v166\n\t"
    "v_and_b32 v165, s45, v166\n\t"
    "v_lshlrev_b32 v166, 16, v167\n\t"
    "v_and_b32 v167, s45, v167\n\t"
    "ds_read_b32 v8, v7\n\t"
    "ds_read_b64_tr_b16 v[128:129], v5 offset:0\n\t"
    "ds_read_b64_tr_b16 v[130:131], v5 offset:3072\n\t"
    "ds_read_b64_tr_b16 v[132:133], v5 offset:384\n\t"
    "ds_read_b64_tr_b16 v[134:135], v5 offset:3456\n\t"
    "ds_read_b64_tr_b16 v[136:137], v5 offset:768\n\t"
    "ds_read_b64_tr_b16 v[138:139], v5 offset:3840\n\t"
    "ds_read_b64_tr_b16 v[140:141], v5 offset:1152\n\t"
    "ds_read_b64_tr_b16 v[142:143], v5 offset:4224\n\t"
    "s_waitcnt lgkmcnt(8)\n\t"
    "v_cmp_lt_u32 vcc, 0, v8\n\t"
    "s_and_b32 s44, vcc_lo, 0xf00000\n\t"
    "s_cbranch_scc1 L_poll46\n\t"
    "L_polld46:\n\t"
    "v_mov_b32 v10, 8\n\t"
    "ds_write_b32 v9, v10\n\t"
    "s_waitcnt lgkmcnt(7)\n\t"
    "v_mfma_f32_16x16x32_bf16 v[128:131], v[12:15], v[128:131], 0\n\t"
    "s_waitcnt lgkmcnt(5)\n\t"
    "v_mfma_f32_16x16x32_bf16 v[132:135], v[12:15], v[132:135], 0\n\t"
    "s_waitcnt lgkmcnt(3)\n\t"
    "v_mfma_f32_16x16x32_bf16 v[136:139], v[12:15], v[136:139], 0\n\t"
    "s_nop 3\n\t"
    "v_cvt_pk_bf16_f32 v96, v128, v129\n\t"
    "v_cvt_pk_bf16_f32 v100, v130, v131\n\t"
    "s_waitcnt lgkmcnt(1)\n\t"
    "v_mfma_f32_16x16x32_bf16 v[140:143], v[12:15], v[140:143], 0\n\t"
    "v_cvt_pk_bf16_f32 v97, v132, v133\n\t"
    "v_cvt_pk_bf16_f32 v101, v134, v135\n\t"
    "v_cvt_pk_bf16_f32 v98, v136, v137\n\t"
    "v_cvt_pk_bf16_f32 v102, v138, v139\n\t"
    "s_nop 3\n\t"
    "v_cvt_pk_bf16_f32 v99, v140, v141\n\t"
    "v_cvt_pk_bf16_f32 v103, v142, v143\n\t"
    "ds_read_b32 v8, v7 offset:256\n\t"
    "ds_read_b128 v[128:131], v120 offset:192\n\t"
    "ds_read_b128 v[140:143], v121 offset:0\n\t"
    "ds_read_b128 v[132:135], v120 offset:256\n\t"
    "ds_read_b128 v[144:147], v121 offset:64\n\t"
    "ds_read_b128 v[136:139], v120 offset:320\n\t"
    "ds_read_b128 v[148:151], v121 offset:128\n\t"
    "s_waitcnt lgkmcnt(6)\n\t"
    "v_cmp_lt_u32 vcc, 0, v8\n\t"
    "s_and_b32 s44, vcc_lo, s48\n\t"
    "s_cbranch_scc1 L_poll47\n\t"
    "L_polld47:\n\t"
    "s_nop 0\n\t"
    "s_waitcnt lgkmcnt(5)\n\t"
    "v_mfma_f32_16x16x32_bf16 v[152:155], v[16:19], v[128:131], v[152:155]\n\t"
    "v_mfma_f32_16x16x32_bf16 v[156:159], v[20:23], v[128:131], v[156:159]\n\t"
    "s_waitcnt lgkmcnt(4)\n\t"
    "v_mfma_f32_16x16x32_bf16 v[160:163], v[16:19], v[140:143], v[160:163]\n\t"
    "v_mfma_f32_16x16x32_bf16 v[164:167], v[20:23], v[140:143], v[164:167]\n\t"
    "s_waitcnt lgkmcnt(3)\n\t"
    "v_mfma_f32_16x16x32_bf16 v[152:155], v[24:27], v[132:135], v[152:155]\n\t"
    "v_lshlrev_b32 v16, 16, v88\n\t"
    "v_and_b32 v17, s45, v88\n\t"
    "v_lshlrev_b32 v18, 16, v89\n\t"
    "v_mfma_f32_16x16x32_bf16 v[156:159], v[28:31], v[132:135], v[156:159]\n\t"
    "v_and_b32 v19, s45, v89\n\t"
    "v_lshlrev_b32 v20, 16, v90\n\t"
    "v_and_b32 v21, s45, v90\n\t"
    "s_waitcnt lgkmcnt(2)\n\t"
    "v_mfma_f32_16x16x32_bf16 v[160:163], v[24:27], v[144:147], v[160:163]\n\t"
    "v_lshlrev_b32 v22, 16, v91\n\t"
    "v_and_b32 v23, s45, v91\n\t"
    "v_mfma_f32_16x16x32_bf16 v[164:167], v[28:31], v[144:147], v[164:167]\n\t"
    "s_waitcnt lgkmcnt(0)\n\t"
    "v_mfma_f32_16x16x32_bf16 v[152:155], v[32:35], v[136:139], v[152:155]\n\t"
    "v_lshlrev_b32 v24, 16, v92\n\t"
    "v_and_b32 v25, s45, v92\n\t"
    "v_lshlrev_b32 v26, 16, v93\n\t"
    "v_mfma_f32_16x16x32_bf16 v[156:159], v[36:39], v[136:139], v[156:159]\n\t"
    "v_and_b32 v27, s45, v93\n\t"
    "v_lshlrev_b32 v28, 16, v94\n\t"
    "v_and_b32 v29, s45, v94\n\t"
    "v_mfma_f32_16x16x32_bf16 v[160:163], v[32:35], v[148:151], v[160:163]\n\t"
    "v_lshlrev_b32 v30, 16, v95\n\t"
    "v_and_b32 v31, s45, v95\n\t"
    "v_mfma_f32_16x16x32_bf16 v[164:167], v[36:39], v[148:151], v[164:167]\n\t"
    "v_mfma_f32_16x16x32_bf16 v[16:19], v[40:43], v[128:131], v[16:19]\n\t"
    "s_nop 3\n\t"
    "v_add_f32 v92, v160, v152\n\t"
    "v_add_f32 v93, v161, v153\n\t"
    "v_cvt_pk_bf16_f32 v88, v92, v93\n\t"
    "v_add_f32 v94, v162, v154\n\t"
    "v_mfma_f32_16x16x32_bf16 v[20:23], v[44:47], v[128:131], v[20:23]\n\t"
    "v_add_f32 v95, v163, v155\n\t"
    "v_cvt_pk_bf16_f32 v89, v94, v95\n\t"
    "v_add_f32 v92, v164, v156\n\t"
    "v_add_f32 v93, v165, v157\n\t"
    "v_mfma_f32_16x16x32_bf16 v[24:27], v[40:43], v[140:143], v[24:27]\n\t"
    "v_cvt_pk_bf16_f32 v90, v92, v93\n\t"
    "v_add_f32 v94, v166, v158\n\t"
    "v_add_f32 v95, v167, v159\n\t"
    "v_cvt_pk_bf16_f32 v91, v94, v95\n\t"
    "v_mfma_f32_16x16x32_bf16 v[28:31], v[44:47], v[140:143], v[28:31]\n\t"
    "ds_write_b128 v0, v[88:91] offset:0\n\t"
    "v_lshlrev_b32 v32, 16, v112\n\t"
    "v_and_b32 v33, s45, v112\n\t"
    "v_mfma_f32_16x16x32_bf16 v[16:19], v[48:51], v[132:135], v[16:19]\n\t"
    "v_lshlrev_b32 v34, 16, v113\n\t"
    "v_and_b32 v35, s45, v113\n\t"
    "v_lshlrev_b32 v36, 16, v114\n\t"
    "v_and_b32 v37, s45, v114\n\t"
    "v_mfma_f32_16x16x32_bf16 v[20:23], v[52:55], v[132:135], v[20:23]\n\t"
    "v_lshlrev_b32 v38, 16, v115\n\t"
    "v_and_b32 v39, s45, v115\n\t"
    "v_lshlrev_b32 v40, 16, v116\n\t"
    "v_and_b32 v41, s45, v116\n\t"
    "v_mfma_f32_16x16x32_bf16 v[24:27], v[48:51], v[144:147], v[24:27]\n\t"
    "v_lshlrev_b32 v42, 16, v117\n\t"
    "v_and_b32 v43, s45, v117\n\t"
    "v_lshlrev_b32 v44, 16, v118\n\t"
    "v_and_b32 v45, s45, v118\n\t"
    "v_mfma_f32_16x16x32_bf16 v[28:31], v[52:55], v[144:147], v[28:31]\n\t"
    "v_lshlrev_b32 v46, 16, v119\n\t"
    "v_and_b32 v47, s45, v119\n\t"
    "v_sub_f32 v93, v160, v152\n\t"
    "v_sub_f32 v94, v161, v153\n\t"
    "v_mfma_f32_16x16x32_bf16 v[16:19], v[56:59], v[136:139], v[16:19]\n\t"
    "v_cvt_pk_bf16_f32 v92, v93, v94\n\t"
    "v_sub_f32 v94, v162, v154\n\t"
    "v_sub_f32 v95, v163, v155\n\t"
    "v_cvt_pk_bf16_f32 v93, v94, v95\n\t"
    "v_mfma_f32_16x16x32_bf16 v[20:23], v[60:63], v[136:139], v[20:23]\n\t"
    "v_sub_f32 v94, v164, v156\n\t"
    "v_sub_f32 v95, v165, v157\n\t"
    "v_cvt_pk_bf16_f32 v94, v94, v95\n\t"
    "v_sub_f32 v95, v166, v158\n\t"
    "v_mfma_f32_16x16x32_bf16 v[24:27], v[56:59], v[148:151], v[24:27]\n\t"
    "v_sub_f32 v10, v167, v159\n\t"
    "v_cvt_pk_bf16_f32 v95, v95, v10\n\t"
    "ds_write_b128 v0, v[92:95] offset:1344\n\t"
    "v_mfma_f32_16x16x32_bf16 v[28:31], v[60:63], v[148:151], v[28:31]\n\t"
    "v_mov_b32 v10, 3\n\t"
    "ds_write_b32 v9, v10 offset:208\n\t"
    "v_mfma_f32_16x16x32_bf16 v[32:35], v[64:67], v[128:131], v[32:35]\n\t"
    "s_nop 0\n\t"
    "v_add_f32 v92, v24, v16\n\t"
    "v_add_f32 v93, v25, v17\n\t"
    "v_cvt_pk_bf16_f32 v88, v92, v93\n\t"
    "v_add_f32 v94, v26, v18\n\t"
    "v_mfma_f32_16x16x32_bf16 v[36:39], v[68:71], v[128:131], v[36:39]\n\t"
    "v_add_f32 v95, v27, v19\n\t"
    "v_cvt_pk_bf16_f32 v89, v94, v95\n\t"
    "v_add_f32 v92, v28, v20\n\t"
    "v_add_f32 v93, v29, v21\n\t"
    "v_mfma_f32_16x16x32_bf16 v[40:43], v[64:67], v[140:143], v[40:43]\n\t"
    "v_cvt_pk_bf16_f32 v90, v92, v93\n\t"
    "v_add_f32 v94, v30, v22\n\t"
    "v_add_f32 v95, v31, v23\n\t"
    "v_cvt_pk_bf16_f32 v91, v94, v95\n\t"
    "v_mfma_f32_16x16x32_bf16 v[44:47], v[68:71], v[140:143], v[44:47]\n\t"
    "ds_write_b128 v0, v[88:91] offset:448\n\t"
    "v_sub_f32 v93, v24, v16\n\t"
    "v_sub_f32 v94, v25, v17\n\t"
    "v_mfma_f32_16x16x32_bf16 v[32:35], v[72:75], v[132:135], v[32:35]\n\t"
    "v_cvt_pk_bf16_f32 v92, v93, v94\n\t"
    "v_sub_f32 v94, v26, v18\n\t"
    "v_sub_f32 v95, v27, v19\n\t"
    "v_cvt_pk_bf16_f32 v93, v94, v95\n\t"
    "v_mfma_f32_16x16x32_bf16 v[36:39], v[76:79], v[132:135], v[36:39]\n\t"
    "v_sub_f32 v94, v28, v20\n\t"
    "v_sub_f32 v95, v29, v21\n\t"
    "v_cvt_pk_bf16_f32 v94, v94, v95\n\t"
    "v_sub_f32 v95, v30, v22\n\t"
    "v_mfma_f32_16x16x32_bf16 v[40:43], v[72:75], v[144:147], v[40:43]\n\t"
    "v_sub_f32 v10, v31, v23\n\t"
    "v_cvt_pk_bf16_f32 v95, v95, v10\n\t"
    "ds_write_b128 v0, v[92:95] offset:1792\n\t"
    "v_mfma_f32_16x16x32_bf16 v[44:47], v[76:79], v[144:147], v[44:47]\n\t"
    "v_mfma_f32_16x16x32_bf16 v[32:35], v[80:83], v[136:139], v[32:35]\n\t"
    "v_mfma_f32_16x16x32_bf16 v[36:39], v[84:87], v[136:139], v[36:39]\n\t"
    "v_mfma_f32_16x16x32_bf16 v[40:43], v[80:83], v[148:151], v[40:43]\n\t"
    "v_mfma_f32_16x16x32_bf16 v[44:47], v[84:87], v[148:151], v[44:47]\n\t"
    "s_nop 6\n\t"
    "v_add_f32 v92, v40, v32\n\t"
    "v_add_f32 v93, v41, v33\n\t"
    "v_cvt_pk_bf16_f32 v88, v92, v93\n\t"
    "v_add_f32 v94, v42, v34\n\t"
    "v_add_f32 v95, v43, v35\n\t"
    "v_cvt_pk_bf16_f32 v89, v94, v95\n\t"
    "v_add_f32 v92, v44, v36\n\t"
    "v_add_f32 v93, v45, v37\n\t"
    "v_cvt_pk_bf16_f32 v90, v92, v93\n\t"
    "v_add_f32 v94, v46, v38\n\t"
    "v_add_f32 v95, v47, v39\n\t"
    "v_cvt_pk_bf16_f32 v91, v94, v95\n\t"
    "ds_write_b128 v0, v[88:91] offset:896\n\t"
    "v_sub_f32 v93, v40, v32\n\t"
    "v_sub_f32 v94, v41, v33\n\t"
    "v_cvt_pk_bf16_f32 v92, v93, v94\n\t"
    "v_sub_f32 v94, v42, v34\n\t"
    "v_sub_f32 v95, v43, v35\n\t"
    "v_cvt_pk_bf16_f32 v93, v94, v95\n\t"
    "v_sub_f32 v94, v44, v36\n\t"
    "v_sub_f32 v95, v45, v37\n\t"
    "v_cvt_pk_bf16_f32 v94, v94, v95\n\t"
    "v_sub_f32 v95, v46, v38\n\t"
    "v_sub_f32 v10, v47, v39\n\t"
    "v_cvt_pk_bf16_f32 v95, v95, v10\n\t"
    "ds_write_b128 v0, v[92:95] offset:2240\n\t"
    "v_mov_b32 v10, 1\n\t"
    "ds_write_b32 v9, v10 offset:208\n\t"
    "s_waitcnt vmcnt(0)\n\t"
    "v_sub_f32_dpp v40, v40, v40 row_ror:1 row_mask:0xf bank_mask:0xf bound_ctrl:0\n\t"
    "v_sub_f32_dpp v41, v41, v41 row_ror:1 row_mask:0xf bank_mask:0xf bound_ctrl:0\n\t"
    "v_sub_f32_dpp v42, v42, v42 row_ror:1 row_mask:0xf bank_mask:0xf bound_ctrl:0\n\t"
    "v_sub_f32_dpp v43, v43, v43 row_ror:1 row_mask:0xf bank_mask:0xf bound_ctrl:0\n\t"
    "v_sub_f32_dpp v44, v44, v44 row_ror:1 row_mask:0xf bank_mask:0xf bound_ctrl:0\n\t"
    "v_sub_f32_dpp v45, v45, v45 row_ror:1 row_mask:0xf bank_mask:0xf bound_ctrl:0\n\t"
    "v_sub_f32_dpp v46, v46, v46 row_ror:1 row_mask:0xf bank_mask:0xf bound_ctrl:0\n\t"
    "v_sub_f32_dpp v47, v47, v47 row_ror:1 row_mask:0xf bank_mask:0xf bound_ctrl:0\n\t"
    "v_cvt_pk_bf16_f32 v40, v40, v41\n\t"
    "v_cvt_pk_bf16_f32 v41, v42, v43\n\t"
    "v_cvt_pk_bf16_f32 v42, v44, v45\n\t"
    "v_cvt_pk_bf16_f32 v43, v46, v47\n\t"
    "ds_write_b128 v0, v[40:43] offset:3584\n\t"
    "v_sub_f32_dpp v32, v32, v32 row_shl:1 row_mask:0xf bank_mask:0xf bound_ctrl:0\n\t"
    "v_sub_f32_dpp v33, v33, v33 row_shl:1 row_mask:0xf bank_mask:0xf bound_ctrl:0\n\t"
    "v_sub_f32_dpp v34, v34, v34 row_shl:1 row_mask:0xf bank_mask:0xf bound_ctrl:0\n\t"
    "v_sub_f32_dpp v35, v35, v35 row_shl:1 row_mask:0xf bank_mask:0xf bound_ctrl:0\n\t"
    "v_sub_f32_dpp v36, v36, v36 row_shl:1 row_mask:0xf bank_mask:0xf bound_ctrl:0\n\t"
    "v_sub_f32_dpp v37, v37, v37 row_shl:1 row_mask:0xf bank_mask:0xf bound_ctrl:0\n\t"
    "v_sub_f32_dpp v38, v38, v38 row_shl:1 row_mask:0xf bank_mask:0xf bound_ctrl:0\n\t"
    "v_sub_f32_dpp v39, v39, v39 row_shl:1 row_mask:0xf bank_mask:0xf bound_ctrl:0\n\t"
    "v_cvt_pk_bf16_f32 v32, v32, v33\n\t"
    "v_cvt_pk_bf16_f32 v33, v34, v35\n\t"
    "v_cvt_pk_bf16_f32 v34, v36, v37\n\t"
    "v_cvt_pk_bf16_f32 v35, v38, v39\n\t"
    "ds_write_b128 v0, v[32:35] offset:4928\n\t"
    "v_sub_f32_dpp v24, v24, v24 row_ror:1 row_mask:0xf bank_mask:0xf bound_ctrl:0\n\t"
    "v_sub_f32_dpp v25, v25, v25 row_ror:1 row_mask:0xf bank_mask:0xf bound_ctrl:0\n\t"
    "v_sub_f32_dpp v26, v26, v26 row_ror:1 row_mask:0xf bank_mask:0xf bound_ctrl:0\n\t"
    "v_sub_f32_dpp v27, v27, v27 row_ror:1 row_mask:0xf bank_mask:0xf bound_ctrl:0\n\t"
    "v_sub_f32_dpp v28, v28, v28 row_ror:1 row_mask:0xf bank_mask:0xf bound_ctrl:0\n\t"
    "v_sub_f32_dpp v29, v29, v29 row_ror:1 row_mask:0xf bank_mask:0xf bound_ctrl:0\n\t"
    "v_sub_f32_dpp v30, v30, v30 row_ror:1 row_mask:0xf bank_mask:0xf bound_ctrl:0\n\t"
    "v_sub_f32_dpp v31, v31, v31 row_ror:1 row_mask:0xf bank_mask:0xf bound_ctrl:0\n\t"
    "v_cvt_pk_bf16_f32 v24, v24, v25\n\t"
    "v_cvt_pk_bf16_f32 v25, v26, v27\n\t"
    "v_cvt_pk_bf16_f32 v26, v28, v29\n\t"
    "v_cvt_pk_bf16_f32 v27, v30, v31\n\t"
    "ds_write_b128 v0, v[24:27] offset:3136\n\t"
    "v_sub_f32_dpp v16, v16, v16 row_shl:1 row_mask:0xf bank_mask:0xf bound_ctrl:0\n\t"
    "v_sub_f32_dpp v17, v17, v17 row_shl:1 row_mask:0xf bank_mask:0xf bound_ctrl:0\n\t"
    "v_sub_f32_dpp v18, v18, v18 row_shl:1 row_mask:0xf bank_mask:0xf bound_ctrl:0\n\t"
    "v_sub_f32_dpp v19, v19, v19 row_shl:1 row_mask:0xf bank_mask:0xf bound_ctrl:0\n\t"
    "v_sub_f32_dpp v20, v20, v20 row_shl:1 row_mask:0xf bank_mask:0xf bound_ctrl:0\n\t"
    "v_sub_f32_dpp v21, v21, v21 row_shl:1 row_mask:0xf bank_mask:0xf bound_ctrl:0\n\t"
    "v_sub_f32_dpp v22, v22, v22 row_shl:1 row_mask:0xf bank_mask:0xf bound_ctrl:0\n\t"
    "v_sub_f32_dpp v23, v23, v23 row_shl:1 row_mask:0xf bank_mask:0xf bound_ctrl:0\n\t"
    "v_cvt_pk_bf16_f32 v16, v16, v17\n\t"
    "v_cvt_pk_bf16_f32 v17, v18, v19\n\t"
    "v_cvt_pk_bf16_f32 v18, v20, v21\n\t"
    "v_cvt_pk_bf16_f32 v19, v22, v23\n\t"
    "ds_write_b128 v0, v[16:19] offset:4480\n\t"
    "v_sub_f32_dpp v160, v160, v160 row_ror:1 row_mask:0xf bank_mask:0xf bound_ctrl:0\n\t"
    "v_sub_f32_dpp v161, v161, v161 row_ror:1 row_mask:0xf bank_mask:0xf bound_ctrl:0\n\t"
    "v_sub_f32_dpp v162, v162, v162 row_ror:1 row_mask:0xf bank_mask:0xf bound_ctrl:0\n\t"
    "v_sub_f32_dpp v163, v163, v163 row_ror:1 row_mask:0xf bank_mask:0xf bound_ctrl:0\n\t"
    "v_sub_f32_dpp v164, v164, v164 row_ror:1 row_mask:0xf bank_mask:0xf bound_ctrl:0\n\t"
    "v_sub_f32_dpp v165, v165, v165 row_ror:1 row_mask:0xf bank_mask:0xf bound_ctrl:0\n\t"
    "v_sub_f32_dpp v166, v166, v166 row_ror:1 row_mask:0xf bank_mask:0xf bound_ctrl:0\n\t"
    "v_sub_f32_dpp v167, v167, v167 row_ror:1 row_mask:0xf bank_mask:0xf bound_ctrl:0\n\t"
    "v_cvt_pk_bf16_f32 v160, v160, v161\n\t"
    "v_cvt_pk_bf16_f32 v161, v162, v163\n\t"
    "v_cvt_pk_bf16_f32 v162, v164, v165\n\t"
    "v_cvt_pk_bf16_f32 v163, v166, v167\n\t"
    "ds_write_b128 v0, v[160:163] offset:2688\n\t"
    "ds_read_b32 v8, v7\n\t"
    "v_sub_f32_dpp v152, v152, v152 row_shl:1 row_mask:0xf bank_mask:0xf bound_ctrl:0\n\t"
    "v_sub_f32_dpp v153, v153, v153 row_shl:1 row_mask:0xf bank_mask:0xf bound_ctrl:0\n\t"
    "v_sub_f32_dpp v154, v154, v154 row_shl:1 row_mask:0xf bank_mask:0xf bound_ctrl:0\n\t"
    "v_sub_f32_dpp v155, v155, v155 row_shl:1 row_mask:0xf bank_mask:0xf bound_ctrl:0\n\t"
    "v_sub_f32_dpp v156, v156, v156 row_shl:1 row_mask:0xf bank_mask:0xf bound_ctrl:0\n\t"
    "v_sub_f32_dpp v157, v157, v157 row_shl:1 row_mask:0xf bank_mask:0xf bound_ctrl:0\n\t"
    "v_sub_f32_dpp v158, v158, v158 row_shl:1 row_mask:0xf bank_mask:0xf bound_ctrl:0\n\t"
    "v_sub_f32_dpp v159, v159, v159 row_shl:1 row_mask:0xf bank_mask:0xf bound_ctrl:0\n\t"
    "v_cvt_pk_bf16_f32 v152, v152, v153\n\t"
    "v_cvt_pk_bf16_f32 v153, v154, v155\n\t"
    "v_cvt_pk_bf16_f32 v154, v156, v157\n\t"
    "v_cvt_pk_bf16_f32 v155, v158, v159\n\t"
    "ds_write_b128 v0, v[152:155] offset:4032\n\t"
    "v_mov_b32 v10, 0\n\t"
    "ds_write_b32 v9, v10 offset:208\n\t"
    "s_setprio 3\n\t"
    "s_waitcnt lgkmcnt(2)\n\t"
    "v_cmp_lt_u32 vcc, 0, v8\n\t"
    "s_and_b32 s44, vcc_lo, 0xf000000\n\t"
    "s_cbranch_scc1 L_slow48\n\t"
    "L_cont48:\n\t"
    "ds_read_b64_tr_b16 v[116:117], v5 offset:18816\n\t"
    "ds_read_b64_tr_b16 v[118:119], v5 offset:21888\n\t"
    "ds_read_b64_tr_b16 v[112:113], v5 offset:19200\n\t"
    "ds_read_b64_tr_b16 v[114:115], v5 offset:22272\n\t"
    "ds_read_b64_tr_b16 v[40:41], v5 offset:19584\n\t"
    "ds_read_b64_tr_b16 v[42:43], v5 offset:22656\n\t"
    "ds_read_b64_tr_b16 v[44:45], v5 offset:19968\n\t"
    "ds_read_b64_tr_b16 v[46:47], v5 offset:23040\n\t"
    "v_mov_b32 v10, 7\n\t"
    "ds_write_b32 v9, v10\n\t"
    "v_cmp_lt_u32 vcc, 0, v8\n\t"
    "s_and_b32 s44, vcc_lo, 0xf0000000\n\t"
    "s_cbranch_scc1 L_slow49\n\t"
    "L_cont49:\n\t"
    "ds_read_b64_tr_b16 v[48:49], v5 offset:37632\n\t"
    "ds_read_b64_tr_b16 v[50:51], v5 offset:40704\n\t"
    "ds_read_b64_tr_b16 v[52:53], v5 offset:38016\n\t"
    "ds_read_b64_tr_b16 v[54:55], v5 offset:41088\n\t"
    "ds_read_b64_tr_b16 v[56:57], v5 offset:38400\n\t"
    "ds_read_b64_tr_b16 v[58:59], v5 offset:41472\n\t"
    "ds_read_b64_tr_b16 v[60:61], v5 offset:38784\n\t"
    "ds_read_b64_tr_b16 v[62:63], v5 offset:41856\n\t"
    "v_mov_b32 v10, 6\n\t"
    "ds_write_b32 v9, v10\n\t"
    "v_cmp_lt_u32 vcc, 3, v8\n\t"
    "s_and_b32 s44, vcc_hi, 0x3f000000\n\t"
    "s_cbranch_scc1 L_slow50\n\t"
    "L_cont50:\n\t"
    "ds_read_b128 v[80:83], v3 offset:0\n\t"
    "ds_read_b128 v[84:87], v3 offset:1344\n\t"
    "s_waitcnt lgkmcnt(11)\n\t"
    "v_mfma_f32_16x16x32_bf16 v[116:119], v[12:15], v[116:119], 0\n\t"
    "ds_read_b128 v[88:91], v3 offset:5376\n\t"
    "v_mfma_f32_16x16x32_bf16 v[112:115], v[12:15], v[112:115], 0\n\t"
    "ds_read_b128 v[92:95], v3 offset:6720\n\t"
    "v_mfma_f32_16x16x32_bf16 v[40:43], v[12:15], v[40:43], 0\n\t"
    "s_nop 3\n\t"
    "v_cvt_pk_bf16_f32 v104, v116, v117\n\t"
    "v_cvt_pk_bf16_f32 v108, v118, v119\n\t"
    "v_mfma_f32_16x16x32_bf16 v[44:47], v[12:15], v[44:47], 0\n\t"
    "v_cvt_pk_bf16_f32 v105, v112, v113\n\t"
    "v_cvt_pk_bf16_f32 v109, v114, v115\n\t"
    "s_waitcnt lgkmcnt(4)\n\t"
    "v_mfma_f32_16x16x32_bf16 v[48:51], v[12:15], v[48:51], 0\n\t"
    "v_cvt_pk_bf16_f32 v106, v40, v41\n\t"
    "v_cvt_pk_bf16_f32 v110, v42, v43\n\t"
    "v_mfma_f32_16x16x32_bf16 v[52:55], v[12:15], v[52:55], 0\n\t"
    "s_nop 0\n\t"
    "v_cvt_pk_bf16_f32 v107, v44, v45\n\t"
    "v_cvt_pk_bf16_f32 v111, v46, v47\n\t"
    "ds_read_b128 v[64:67], v3 offset:10752\n\t"
    "v_mfma_f32_16x16x32_bf16 v[56:59], v[12:15], v[56:59], 0\n\t"
    "v_cvt_pk_bf16_f32 v112, v48, v49\n\t"
    "v_cvt_pk_bf16_f32 v116, v50, v51\n\t"
    "v_mfma_f32_16x16x32_bf16 v[60:63], v[12:15], v[60:63], 0\n\t"
    "v_cvt_pk_bf16_f32 v113, v52, v53\n\t"
    "v_cvt_pk_bf16_f32 v117, v54, v55\n\t"
    "s_waitcnt lgkmcnt(3)\n\t"
    "v_mfma_f32_16x16x32_bf16 v[40:43], v[96:99], v[80:83], 0\n\t"
    "s_nop 0\n\t"
    "v_cvt_pk_bf16_f32 v114, v56, v57\n\t"
    "v_cvt_pk_bf16_f32 v118, v58, v59\n\t"
    "ds_read_b128 v[68:71], v3 offset:12096\n\t"
    "v_mfma_f32_16x16x32_bf16 v[44:47], v[100:103], v[84:87], 0\n\t"
    "v_cvt_pk_bf16_f32 v115, v60, v61\n\t"
    "v_cvt_pk_bf16_f32 v119, v62, v63\n\t"
    "s_waitcnt lgkmcnt(2)\n\t"
    "v_mfma_f32_16x16x32_bf16 v[40:43], v[104:107], v[88:91], v[40:43]\n\t"
    "v_mfma_f32_16x16x32_bf16 v[48:51], v[96:99], v[88:91], 0\n\t"
    "ds_read_b128 v[72:75], v3 offset:16128\n\t"
    "v_mfma_f32_16x16x32_bf16 v[44:47], v[108:111], v[92:95], v[44:47]\n\t"
    "ds_read_b32 v8, v7\n\t"
    "v_mfma_f32_16x16x32_bf16 v[52:55], v[100:103], v[92:95], 0\n\t"
    "s_waitcnt lgkmcnt(3)\n\t"
    "v_mfma_f32_16x16x32_bf16 v[40:43], v[112:115], v[64:67], v[40:43]\n\t"
    "ds_read_b128 v[76:79], v3 offset:17472\n\t"
    "v_mfma_f32_16x16x32_bf16 v[48:51], v[104:107], v[64:67], v[48:51]\n\t"
    "v_mfma_f32_16x16x32_bf16 v[56:59], v[96:99], v[64:67], 0\n\t"
    "s_waitcnt lgkmcnt(3)\n\t"
    "v_mfma_f32_16x16x32_bf16 v[44:47], v[116:119], v[68:71], v[44:47]\n\t"
    "ds_read_b128 v[120:123], v3 offset:21504\n\t"
    "s_waitcnt lgkmcnt(2)\n\t"
    "v_cmp_lt_u32 vcc, 0, v8\n\t"
    "v_mfma_f32_16x16x32_bf16 v[52:55], v[108:111], v[68:71], v[52:55]\n\t"
    "s_and_b32 s44, vcc_hi, 0xf\n\t"
    "s_cbranch_scc1 L_slow51\n\t"
    "L_cont51:\n\t"
    "ds_read_b64_tr_b16 v[80:81], v5 offset:56448\n\t"
    "v_mfma_f32_16x16x32_bf16 v[60:63], v[100:103], v[68:71], 0\n\t"
    "ds_read_b64_tr_b16 v[82:83], v5 offset:59520\n\t"
    "ds_read_b64_tr_b16 v[84:85], v5 offset:56832\n\t"
    "ds_read_b64_tr_b16 v[86:87], v5 offset:59904\n\t"
    "v_mfma_f32_16x16x32_bf16 v[48:51], v[112:115], v[72:75], v[48:51]\n\t"
    "ds_read_b64_tr_b16 v[88:89], v5 offset:57216\n\t"
    "ds_read_b64_tr_b16 v[90:91], v5 offset:60288\n\t"
    "ds_read_b64_tr_b16 v[92:93], v5 offset:57600\n\t"
    "ds_read_b128 v[124:127], v3 offset:22848\n\t"
    "ds_read_b32 v8, v7\n\t"
    "v_mfma_f32_16x16x32_bf16 v[56:59], v[104:107], v[72:75], v[56:59]\n\t"
    "ds_read_b64_tr_b16 v[94:95], v5 offset:60672\n\t"
    "v_mov_b32 v10, 5\n\t"
    "ds_write_b32 v9, v10\n\t"
    "v_mfma_f32_16x16x32_bf16 v[64:67], v[96:99], v[72:75], 0\n\t"
    "s_waitcnt lgkmcnt(12)\n\t"
    "v_mfma_f32_16x16x32_bf16 v[52:55], v[116:119], v[76:79], v[52:55]\n\t"
    "v_mfma_f32_16x16x32_bf16 v[60:63], v[108:111], v[76:79], v[60:63]\n\t"
    "v_mfma_f32_16x16x32_bf16 v[68:71], v[100:103], v[76:79], 0\n\t"
    "s_waitcnt lgkmcnt(2)\n\t"
    "v_cmp_lt_u32 vcc, 1, v8\n\t"
    "v_mfma_f32_16x16x32_bf16 v[56:59], v[112:115], v[120:123], v[56:59]\n\t"
    "s_and_b32 s44, vcc_hi, 0xff000000\n\t"
    "s_cbranch_scc1 L_slow52\n\t"
    "L_cont52:\n\t"
    "v_mfma_f32_16x16x32_bf16 v[64:67], v[104:107], v[120:123], v[64:67]\n\t"
    "ds_read_b128 v[148:151], v3 offset:26880\n\t"
    "v_mfma_f32_16x16x32_bf16 v[72:75], v[96:99], v[120:123], 0\n\t"
    "ds_read_b32 v8, v7\n\t"
    "v_mfma_f32_16x16x32_bf16 v[60:63], v[116:119], v[124:127], v[60:63]\n\t"
    "v_mfma_f32_16x16x32_bf16 v[68:71], v[108:111], v[124:127], v[68:71]\n\t"
    "ds_read_b128 v[152:155], v3 offset:28224\n\t"
    "v_mfma_f32_16x16x32_bf16 v[76:79], v[100:103], v[124:127], 0\n\t"
    "ds_read_b128 v[156:159], v3 offset:448\n\t"
    "v_mfma_f32_16x16x32_bf16 v[80:83], v[12:15], v[80:83], 0\n\t"
    "ds_read_b128 v[160:163], v3 offset:1792\n\t"
    "v_mfma_f32_16x16x32_bf16 v[84:87], v[12:15], v[84:87], 0\n\t"
    "ds_read_b128 v[164:167], v3 offset:32256\n\t"
    "s_waitcnt lgkmcnt(4)\n\t"
    "v_cmp_lt_u32 vcc, 0, v8\n\t"
    "v_mfma_f32_16x16x32_bf16 v[88:91], v[12:15], v[88:91], 0\n\t"
    "s_nop 1\n\t"
    "v_cvt_pk_bf16_f32 v120, v80, v81\n\t"
    "v_cvt_pk_bf16_f32 v124, v82, v83\n\t"
    "s_and_b32 s44, vcc_hi, 0xf0\n\t"
    "s_cbranch_scc1 L_slow53\n\t"
    "L_cont53:\n\t"
    "v_mfma_f32_16x16x32_bf16 v[92:95], v[12:15], v[92:95], 0\n\t"
    "v_cvt_pk_bf16_f32 v121, v84, v85\n\t"
    "v_cvt_pk_bf16_f32 v125, v86, v87\n\t"
    "ds_read_b64_tr_b16 v[136:137], v5 offset:0\n\t"
    "v_mfma_f32_16x16x32_bf16 v[64:67], v[112:115], v[148:151], v[64:67]\n\t"
    "v_cvt_pk_bf16_f32 v122, v88, v89\n\t"
    "v_cvt_pk_bf16_f32 v126, v90, v91\n\t"
    "ds_read_b64_tr_b16 v[138:139], v5 offset:3072\n\t"
    "v_mfma_f32_16x16x32_bf16 v[72:75], v[104:107], v[148:151], v[72:75]\n\t"
    "v_cvt_pk_bf16_f32 v123, v92, v93\n\t"
    "v_cvt_pk_bf16_f32 v127, v94, v95\n\t"
    "ds_read_b64_tr_b16 v[140:141], v5 offset:384\n\t"
    "v_mfma_f32_16x16x32_bf16 v[80:83], v[96:99], v[148:151], 0\n\t"
    "ds_read_b64_tr_b16 v[142:143], v5 offset:3456\n\t"
    "ds_read_b64_tr_b16 v[148:149], v5 offset:768\n\t"
    "ds_read_b64_tr_b16 v[150:151], v5 offset:3840\n\t"
    "ds_read_b128 v[132:135], v3 offset:33600\n\t"
    "s_waitcnt lgkmcnt(10)\n\t"
    "v_mfma_f32_16x16x32_bf16 v[68:71], v[116:119], v[152:155], v[68:71]\n\t"
    "ds_read_b64_tr_b16 v[144:145], v5 offset:1152\n\t"
    "ds_read_b64_tr_b16 v[146:147], v5 offset:4224\n\t"
    "v_mov_b32 v10, 4\n\t"
    "ds_write_b32 v9, v10\n\t"
    "v_mfma_f32_16x16x32_bf16 v[76:79], v[108:111], v[152:155], v[76:79]\n\t"
    "v_mfma_f32_16x16x32_bf16 v[84:87], v[100:103], v[152:155], 0\n\t"
    "s_waitcnt lgkmcnt(10)\n\t"
    "v_mfma_f32_16x16x32_bf16 v[40:43], v[120:123], v[156:159], v[40:43]\n\t"
    "ds_read_b32 v8, v7\n\t"
    "v_mfma_f32_16x16x32_bf16 v[44:47], v[124:127], v[160:163], v[44:47]\n\t"
    "v_mfma_f32_16x16x32_bf16 v[72:75], v[112:115], v[164:167], v[72:75]\n\t"
    "v_mfma_f32_16x16x32_bf16 v[80:83], v[104:107], v[164:167], v[80:83]\n\t"
    "ds_read_b128 v[16:19], v3 offset:37632\n\t"
    "v_mfma_f32_16x16x32_bf16 v[88:91], v[96:99], v[164:167], 0\n\t"
    "ds_read_b128 v[20:23], v3 offset:38976\n\t"
    "s_waitcnt lgkmcnt(6)\n\t"
    "v_mfma_f32_16x16x32_bf16 v[76:79], v[116:119], v[132:135], v[76:79]\n\t"
    "ds_read_b128 v[96:99], v3 offset:5824\n\t"
    "s_waitcnt lgkmcnt(3)\n\t"
    "v_cmp_lt_u32 vcc, 0, v8\n\t"
    "v_mfma_f32_16x16x32_bf16 v[84:87], v[108:111], v[132:135], v[84:87]\n\t"
    "s_and_b32 s44, vcc_hi, 0xf00\n\t"
    "s_cbranch_scc1 L_slow54\n\t"
    "L_cont54:\n\t"
    "ds_read_b64_tr_b16 v[152:153], v5 offset:18816\n\t"
    "v_mfma_f32_16x16x32_bf16 v[92:95], v[100:103], v[132:135], 0\n\t"
    "ds_read_b64_tr_b16 v[154:155], v5 offset:21888\n\t"
    "ds_read_b64_tr_b16 v[156:157], v5 offset:19200\n\t"
    "ds_read_b64_tr_b16 v[158:159], v5 offset:22272\n\t"
    "v_mfma_f32_16x16x32_bf16 v[136:139], v[12:15], v[136:139], 0\n\t"
    "ds_read_b64_tr_b16 v[160:161], v5 offset:19584\n\t"
    "ds_read_b64_tr_b16 v[162:163], v5 offset:22656\n\t"
    "ds_read_b64_tr_b16 v[164:165], v5 offset:19968\n\t"
    "v_mfma_f32_16x16x32_bf16 v[140:143], v[12:15], v[140:143], 0\n\t"
    "ds_read_b64_tr_b16 v[166:167], v5 offset:23040\n\t"
    "ds_read_b128 v[100:103], v3 offset:7168\n\t"
    "v_mfma_f32_16x16x32_bf16 v[148:151], v[12:15], v[148:151], 0\n\t"
    "s_nop 0\n\t"
    "v_cvt_pk_bf16_f32 v128, v136, v137\n\t"
    "v_cvt_pk_bf16_f32 v132, v138, v139\n\t"
    "v_mfma_f32_16x16x32_bf16 v[144:147], v[12:15], v[144:147], 0\n\t"
    "s_nop 0\n\t"
    "v_cvt_pk_bf16_f32 v129, v140, v141\n\t"
    "v_cvt_pk_bf16_f32 v133, v142, v143\n\t"
    "s_waitcnt lgkmcnt(11)\n\t"
    "v_mfma_f32_16x16x32_bf16 v[80:83], v[112:115], v[16:19], v[80:83]\n\t"
    "v_cvt_pk_bf16_f32 v130, v148, v149\n\t"
    "v_cvt_pk_bf16_f32 v134, v150, v151\n\t"
    "v_mfma_f32_16x16x32_bf16 v[88:91], v[104:107], v[16:19], v[88:91]\n\t"
    "v_cvt_pk_bf16_f32 v131, v144, v145\n\t"
    "v_cvt_pk_bf16_f32 v135, v146, v147\n\t"
    "s_waitcnt lgkmcnt(9)\n\t"
    "v_mfma_f32_16x16x32_bf16 v[84:87], v[116:119], v[20:23], v[84:87]\n\t"
    "v_mfma_f32_16x16x32_bf16 v[92:95], v[108:111], v[20:23], v[92:95]\n\t"
    "ds_read_b128 v[148:151], v3 offset:43008\n\t"
    "v_mfma_f32_16x16x32_bf16 v[40:43], v[128:131], v[96:99], v[40:43]\n\t"
    "ds_read_b128 v[144:147], v3 offset:44352\n\t"
    "v_mfma_f32_16x16x32_bf16 v[48:51], v[120:123], v[96:99], v[48:51]\n\t"
    "s_waitcnt lgkmcnt(2)\n\t"
    "v_mfma_f32_16x16x32_bf16 v[44:47], v[132:135], v[100:103], v[44:47]\n\t"
    "ds_read_b128 v[108:111], v3 offset:11200\n\t"
    "v_mfma_f32_16x16x32_bf16 v[52:55], v[124:127], v[100:103], v[52:55]\n\t"
    "v_mfma_f32_16x16x32_bf16 v[152:155], v[12:15], v[152:155], 0\n\t"
    "ds_read_b32 v8, v7\n\t"
    "v_mfma_f32_16x16x32_bf16 v[156:159], v[12:15], v[156:159], 0\n\t"
    "ds_read_b128 v[104:107], v3 offset:12544\n\t"
    "v_mfma_f32_16x16x32_bf16 v[160:163], v[12:15], v[160:163], 0\n\t"
    "s_nop 3\n\t"
    "v_cvt_pk_bf16_f32 v136, v152, v153\n\t"
    "v_cvt_pk_bf16_f32 v140, v154, v155\n\t"
    "v_mfma_f32_16x16x32_bf16 v[164:167], v[12:15], v[164:167], 0\n\t"
    "v_cvt_pk_bf16_f32 v137, v156, v157\n\t"
    "v_cvt_pk_bf16_f32 v141, v158, v159\n\t"
    "s_waitcnt lgkmcnt(4)\n\t"
    "v_mfma_f32_16x16x32_bf16 v[88:91], v[112:115], v[148:151], v[88:91]\n\t"
    "v_cvt_pk_bf16_f32 v138, v160, v161\n\t"
    "v_cvt_pk_bf16_f32 v142, v162, v163\n\t"
    "ds_read_b128 v[100:103], v3 offset:16576\n\t"
    "s_waitcnt lgkmcnt(4)\n\t"
    "v_mfma_f32_16x16x32_bf16 v[92:95], v[116:119], v[144:147], v[92:95]\n\t"
    "v_cvt_pk_bf16_f32 v139, v164, v165\n\t"
    "v_cvt_pk_bf16_f32 v143, v166, v167\n\t"
    "s_waitcnt lgkmcnt(2)\n\t"
    "v_cmp_lt_u32 vcc, 0, v8\n\t"
    "s_and_b32 s44, vcc_hi, 0xf000\n\t"
    "s_cbranch_scc1 L_slow55\n\t"
    "L_cont55:\n\t"
    "ds_read_b64_tr_b16 v[156:157], v5 offset:37632\n\t"
    "v_mfma_f32_16x16x32_bf16 v[40:43], v[136:139], v[108:111], v[40:43]\n\t"
    "ds_read_b64_tr_b16 v[158:159], v5 offset:40704\n\t"
    "ds_read_b64_tr_b16 v[152:153], v5 offset:38016\n\t"
    "ds_read_b64_tr_b16 v[154:155], v5 offset:41088\n\t"
    "ds_read_b128 v[116:119], v3 offset:17920\n\t"
    "v_mfma_f32_16x16x32_bf16 v[48:51], v[128:131], v[108:111], v[48:51]\n\t"
    "ds_read_b64_tr_b16 v[164:165], v5 offset:38400\n\t"
    "ds_read_b64_tr_b16 v[166:167], v5 offset:41472\n\t"
    "ds_read_b64_tr_b16 v[160:161], v5 offset:38784\n\t"
    "v_mfma_f32_16x16x32_bf16 v[56:59], v[120:123], v[108:111], v[56:59]\n\t"
    "ds_read_b64_tr_b16 v[162:163], v5 offset:41856\n\t"
    "s_waitcnt lgkmcnt(10)\n\t"
    "v_mfma_f32_16x16x32_bf16 v[44:47], v[140:143], v[104:107], v[44:47]\n\t"
    "v_mfma_f32_16x16x32_bf16 v[52:55], v[132:135], v[104:107], v[52:55]\n\t"
    "v_mfma_f32_16x16x32_bf16 v[60:63], v[124:127], v[104:107], v[60:63]\n\t"
    "s_waitcnt lgkmcnt(9)\n\t"
    "v_mfma_f32_16x16x32_bf16 v[48:51], v[136:139], v[100:103], v[48:51]\n\t"
    "v_mfma_f32_16x16x32_bf16 v[56:59], v[128:131], v[100:103], v[56:59]\n\t"
    "ds_read_b128 v[108:111], v3 offset:21952\n\t"
    "ds_read_b32 v8, v7\n\t"
    "v_mfma_f32_16x16x32_bf16 v[64:67], v[120:123], v[100:103], v[64:67]\n\t"
    "s_waitcnt lgkmcnt(6)\n\t"
    "v_mfma_f32_16x16x32_bf16 v[52:55], v[140:143], v[116:119], v[52:55]\n\t"
    "v_mfma_f32_16x16x32_bf16 v[60:63], v[132:135], v[116:119], v[60:63]\n\t"
    "ds_read_b128 v[104:107], v3 offset:23296\n\t"
    "v_mfma_f32_16x16x32_bf16 v[68:71], v[124:127], v[116:119], v[68:71]\n\t"
    "ds_read_b128 v[100:103], v3 offset:896\n\t"
    "v_mfma_f32_16x16x32_bf16 v[156:159], v[12:15], v[156:159], 0\n\t"
    "ds_read_b128 v[96:99], v3 offset:2240\n\t"
    "s_waitcnt lgkmcnt(3)\n\t"
    "v_cmp_lt_u32 vcc, 0, v8\n\t"
    "v_mfma_f32_16x16x32_bf16 v[152:155], v[12:15], v[152:155], 0\n\t"
    "s_and_b32 s44, vcc_hi, 0xf0000\n\t"
    "s_cbranch_scc1 L_slow56\n\t"
    "L_cont56:\n\t"
    "ds_read_b64_tr_b16 v[116:117], v5 offset:56448\n\t"
    "v_mfma_f32_16x16x32_bf16 v[164:167], v[12:15], v[164:167], 0\n\t"
    "v_cvt_pk_bf16_f32 v144, v156, v157\n\t"
    "v_cvt_pk_bf16_f32 v148, v158, v159\n\t"
    "ds_read_b64_tr_b16 v[118:119], v5 offset:59520\n\t"
    "v_mfma_f32_16x16x32_bf16 v[160:163], v[12:15], v[160:163], 0\n\t"
    "v_cvt_pk_bf16_f32 v145, v152, v153\n\t"
    "v_cvt_pk_bf16_f32 v149, v154, v155\n\t"
    "ds_read_b64_tr_b16 v[112:113], v5 offset:56832\n\t"
    "ds_read_b128 v[20:23], v3 offset:27328\n\t"
    "s_setprio 2\n\t"
    "v_mfma_f32_16x16x32_bf16 v[56:59], v[136:139], v[108:111], v[56:59]\n\t"
    "v_cvt_pk_bf16_f32 v146, v164, v165\n\t"
    "v_cvt_pk_bf16_f32 v150, v166, v167\n\t"
    "ds_read_b64_tr_b16 v[114:115], v5 offset:59904\n\t"
    "v_mfma_f32_16x16x32_bf16 v[64:67], v[128:131], v[108:111], v[64:67]\n\t"
    "v_cvt_pk_bf16_f32 v147, v160, v161\n\t"
    "v_cvt_pk_bf16_f32 v151, v162, v163\n\t"
    "ds_read_b64_tr_b16 v[164:165], v5 offset:57216\n\t"
    "v_mfma_f32_16x16x32_bf16 v[72:75], v[120:123], v[108:111], v[72:75]\n\t"
    "ds_read_b64_tr_b16 v[166:167], v5 offset:60288\n\t"
    "ds_read_b64_tr_b16 v[160:161], v5 offset:57600\n\t"
    "ds_read_b64_tr_b16 v[162:163], v5 offset:60672\n\t"
    "ds_read_b128 v[152:155], v3 offset:28672\n\t"
    "s_waitcnt lgkmcnt(12)\n\t"
    "v_mfma_f32_16x16x32_bf16 v[60:63], v[140:143], v[104:107], v[60:63]\n\t"
    "v_mov_b32 v10, 3\n\t"
    "ds_write_b32 v9, v10\n\t"
    "v_mfma_f32_16x16x32_bf16 v[68:71], v[132:135], v[104:107], v[68:71]\n\t"
    "v_mfma_f32_16x16x32_bf16 v[76:79], v[124:127], v[104:107], v[76:79]\n\t"
    "ds_read_b32 v8, v7\n\t"
    "s_waitcnt lgkmcnt(11)\n\t"
    "v_mfma_f32_16x16x32_bf16 v[40:43], v[144:147], v[100:103], v[40:43]\n\t"
    "v_mfma_f32_16x16x32_bf16 v[44:47], v[148:151], v[96:99], v[44:47]\n\t"
    "s_waitcnt lgkmcnt(8)\n\t"
    "v_mfma_f32_16x16x32_bf16 v[64:67], v[136:139], v[20:23], v[64:67]\n\t"
    "v_mfma_f32_16x16x32_bf16 v[72:75], v[128:131], v[20:23], v[72:75]\n\t"
    "ds_read_b128 v[16:19], v3 offset:32704\n\t"
    "v_mfma_f32_16x16x32_bf16 v[80:83], v[120:123], v[20:23], v[80:83]\n\t"
    "ds_read_b128 v[20:23], v3 offset:34048\n\t"
    "s_waitcnt lgkmcnt(2)\n\t"
    "v_cmp_lt_u32 vcc, 0, v8\n\t"
    "v_mfma_f32_16x16x32_bf16 v[68:71], v[140:143], v[152:155], v[68:71]\n\t"
    "s_and_b32 s44, vcc_hi, 0xf00000\n\t"
    "s_cbranch_scc1 L_slow57\n\t"
    "L_cont57:\n\t"
    "ds_read_b64_tr_b16 v[108:109], v5 offset:0\n\t"
    "v_mfma_f32_16x16x32_bf16 v[76:79], v[132:135], v[152:155], v[76:79]\n\t"
    "ds_read_b64_tr_b16 v[110:111], v5 offset:3072\n\t"
    "ds_read_b64_tr_b16 v[104:105], v5 offset:384\n\t"
    "ds_read_b64_tr_b16 v[106:107], v5 offset:3456\n\t"
    "v_mfma_f32_16x16x32_bf16 v[84:87], v[124:127], v[152:155], v[84:87]\n\t"
    "ds_read_b64_tr_b16 v[100:101], v5 offset:768\n\t"
    "ds_read_b64_tr_b16 v[102:103], v5 offset:3840\n\t"
    "ds_read_b64_tr_b16 v[96:97], v5 offset:1152\n\t"
    "v_mfma_f32_16x16x32_bf16 v[116:119], v[12:15], v[116:119], 0\n\t"
    "ds_read_b64_tr_b16 v[98:99], v5 offset:4224\n\t"
    "v_mfma_f32_16x16x32_bf16 v[112:115], v[12:15], v[112:115], 0\n\t"
    "v_mfma_f32_16x16x32_bf16 v[164:167], v[12:15], v[164:167], 0\n\t"
    "s_nop 4\n\t"
    "v_cvt_pk_bf16_f32 v152, v116, v117\n\t"
    "v_cvt_pk_bf16_f32 v156, v118, v119\n\t"
    "v_mfma_f32_16x16x32_bf16 v[160:163], v[12:15], v[160:163], 0\n\t"
    "v_cvt_pk_bf16_f32 v153, v112, v113\n\t"
    "v_cvt_pk_bf16_f32 v157, v114, v115\n\t"
    "s_waitcnt lgkmcnt(9)\n\t"
    "v_mfma_f32_16x16x32_bf16 v[72:75], v[136:139], v[16:19], v[72:75]\n\t"
    "v_cvt_pk_bf16_f32 v154, v164, v165\n\t"
    "v_cvt_pk_bf16_f32 v158, v166, v167\n\t"
    "v_mfma_f32_16x16x32_bf16 v[80:83], v[128:131], v[16:19], v[80:83]\n\t"
    "s_nop 0\n\t"
    "v_cvt_pk_bf16_f32 v155, v160, v161\n\t"
    "v_cvt_pk_bf16_f32 v159, v162, v163\n\t"
    "ds_read_b128 v[116:119], v3 offset:6272\n\t"
    "v_mfma_f32_16x16x32_bf16 v[88:91], v[120:123], v[16:19], v[88:91]\n\t"
    "s_waitcnt lgkmcnt(5)\n\t"
    "v_mfma_f32_16x16x32_bf16 v[76:79], v[140:143], v[20:23], v[76:79]\n\t"
    "ds_read_b128 v[112:115], v3 offset:7616\n\t"
    "v_mfma_f32_16x16x32_bf16 v[84:87], v[132:135], v[20:23], v[84:87]\n\t"
    "v_mfma_f32_16x16x32_bf16 v[92:95], v[124:127], v[20:23], v[92:95]\n\t"
    "ds_read_b128 v[16:19], v3 offset:11648\n\t"
    "v_mfma_f32_16x16x32_bf16 v[108:111], v[12:15], v[108:111], 0\n\t"
    "v_mfma_f32_16x16x32_bf16 v[104:107], v[12:15], v[104:107], 0\n\t"
    "ds_read_b128 v[20:23], v3 offset:12992\n\t"
    "s_waitcnt lgkmcnt(4)\n\t"
    "v_mfma_f32_16x16x32_bf16 v[100:103], v[12:15], v[100:103], 0\n\t"
    "s_nop 3\n\t"
    "v_cvt_pk_bf16_f32 v160, v108, v109\n\t"
    "v_cvt_pk_bf16_f32 v164, v110, v111\n\t"
    "v_mfma_f32_16x16x32_bf16 v[96:99], v[12:15], v[96:99], 0\n\t"
    "v_cvt_pk_bf16_f32 v161, v104, v105\n\t"
    "v_cvt_pk_bf16_f32 v165, v106, v107\n\t"
    "s_waitcnt lgkmcnt(3)\n\t"
    "v_mfma_f32_16x16x32_bf16 v[40:43], v[152:155], v[116:119], v[40:43]\n\t"
    "v_cvt_pk_bf16_f32 v162, v100, v101\n\t"
    "v_cvt_pk_bf16_f32 v166, v102, v103\n\t"
    "v_mfma_f32_16x16x32_bf16 v[48:51], v[144:147], v[116:119], v[48:51]\n\t"
    "s_nop 0\n\t"
    "v_cvt_pk_bf16_f32 v163, v96, v97\n\t"
    "v_cvt_pk_bf16_f32 v167, v98, v99\n\t"
    "ds_read_b128 v[108:111], v3 offset:17024\n\t"
    "s_waitcnt lgkmcnt(3)\n\t"
    "v_mfma_f32_16x16x32_bf16 v[44:47], v[156:159], v[112:115], v[44:47]\n\t"
    "v_mfma_f32_16x16x32_bf16 v[52:55], v[148:151], v[112:115], v[52:55]\n\t"
    "ds_read_b32 v8, v7\n\t"
    "s_waitcnt lgkmcnt(3)\n\t"
    "v_mfma_f32_16x16x32_bf16 v[40:43], v[160:163], v[16:19], v[40:43]\n\t"
    "ds_read_b128 v[96:99], v3 offset:18368\n\t"
    "v_mfma_f32_16x16x32_bf16 v[48:51], v[152:155], v[16:19], v[48:51]\n\t"
    "v_mfma_f32_16x16x32_bf16 v[56:59], v[144:147], v[16:19], v[56:59]\n\t"
    "s_waitcnt lgkmcnt(3)\n\t"
    "v_mfma_f32_16x16x32_bf16 v[44:47], v[164:167], v[20:23], v[44:47]\n\t"
    "ds_read_b128 v[112:115], v3 offset:22400\n\t"
    "v_mfma_f32_16x16x32_bf16 v[52:55], v[156:159], v[20:23], v[52:55]\n\t"
    "ds_read_b128 v[16:19], v3 offset:23744\n\t"
    "s_waitcnt lgkmcnt(3)\n\t"
    "v_cmp_lt_u32 vcc, 3, v8\n\t"
    "v_mfma_f32_16x16x32_bf16 v[60:63], v[148:151], v[20:23], v[60:63]\n\t"
    "s_and_b32 s44, vcc_lo, 0xfff\n\t"
    "s_cbranch_scc1 L_slow58\n\t"
    "L_cont58:\n\t"
    "v_mfma_f32_16x16x32_bf16 v[48:51], v[160:163], v[108:111], v[48:51]\n\t"
    "v_cvt_pk_bf16_f32 v40, v40, v41\n\t"
    "v_cvt_pk_bf16_f32 v41, v42, v43\n\t"
    "v_mfma_f32_16x16x32_bf16 v[56:59], v[152:155], v[108:111], v[56:59]\n\t"
    "v_cvt_pk_bf16_f32 v42, v44, v45\n\t"
    "v_cvt_pk_bf16_f32 v43, v46, v47\n\t"
    "v_mfma_f32_16x16x32_bf16 v[64:67], v[144:147], v[108:111], v[64:67]\n\t"
    "ds_write_b128 v11, v[40:43] offset:18816\n\t"
    "s_waitcnt lgkmcnt(3)\n\t"
    "v_mfma_f32_16x16x32_bf16 v[52:55], v[164:167], v[96:99], v[52:55]\n\t"
    "v_mov_b32 v10, 2\n\t"
    "ds_write_b32 v9, v10\n\t"
    "ds_read_b128 v[20:23], v3 offset:27776\n\t"
    "v_mfma_f32_16x16x32_bf16 v[60:63], v[156:159], v[96:99], v[60:63]\n\t"
    "ds_read_b128 v[40:43], v3 offset:29120\n\t"
    "v_mfma_f32_16x16x32_bf16 v[68:71], v[148:151], v[96:99], v[68:71]\n\t"
    "v_cvt_pk_bf16_f32 v48, v48, v49\n\t"
    "v_cvt_pk_bf16_f32 v49, v50, v51\n\t"
    "s_waitcnt lgkmcnt(5)\n\t"
    "v_mfma_f32_16x16x32_bf16 v[56:59], v[160:163], v[112:115], v[56:59]\n\t"
    "v_cvt_pk_bf16_f32 v50, v52, v53\n\t"
    "v_cvt_pk_bf16_f32 v51, v54, v55\n\t"
    "v_mfma_f32_16x16x32_bf16 v[64:67], v[152:155], v[112:115], v[64:67]\n\t"
    "ds_write_b128 v11, v[48:51] offset:21504\n\t"
    "v_mfma_f32_16x16x32_bf16 v[72:75], v[144:147], v[112:115], v[72:75]\n\t"
    "v_mov_b32 v10, 1\n\t"
    "ds_write_b32 v9, v10\n\t"
    "s_waitcnt lgkmcnt(6)\n\t"
    "v_mfma_f32_16x16x32_bf16 v[60:63], v[164:167], v[16:19], v[60:63]\n\t"
    "ds_read_b128 v[52:55], v3 offset:33152\n\t"
    "s_setprio 1\n\t"
    "v_mfma_f32_16x16x32_bf16 v[68:71], v[156:159], v[16:19], v[68:71]\n\t"
    "ds_read_b128 v[48:51], v3 offset:34496\n\t"
    "v_mfma_f32_16x16x32_bf16 v[76:79], v[148:151], v[16:19], v[76:79]\n\t"
    "v_cvt_pk_bf16_f32 v56, v56, v57\n\t"
    "v_cvt_pk_bf16_f32 v57, v58, v59\n\t"
    "s_waitcnt lgkmcnt(5)\n\t"
    "v_mfma_f32_16x16x32_bf16 v[64:67], v[160:163], v[20:23], v[64:67]\n\t"
    "v_cvt_pk_bf16_f32 v58, v60, v61\n\t"
    "v_cvt_pk_bf16_f32 v59, v62, v63\n\t"
    "v_mfma_f32_16x16x32_bf16 v[72:75], v[152:155], v[20:23], v[72:75]\n\t"
    "ds_write_b128 v11, v[56:59] offset:24192\n\t"
    "v_mfma_f32_16x16x32_bf16 v[80:83], v[144:147], v[20:23], v[80:83]\n\t"
    "s_waitcnt lgkmcnt(5)\n\t"
    "v_mfma_f32_16x16x32_bf16 v[68:71], v[164:167], v[40:43], v[68:71]\n\t"
    "ds_read_b128 v[60:63], v3 offset:38080\n\t"
    "v_mfma_f32_16x16x32_bf16 v[76:79], v[156:159], v[40:43], v[76:79]\n\t"
    "ds_read_b128 v[20:23], v3 offset:39424\n\t"
    "v_mfma_f32_16x16x32_bf16 v[84:87], v[148:151], v[40:43], v[84:87]\n\t"
    "v_cvt_pk_bf16_f32 v64, v64, v65\n\t"
    "v_cvt_pk_bf16_f32 v65, v66, v67\n\t"
    "s_waitcnt lgkmcnt(4)\n\t"
    "v_mfma_f32_16x16x32_bf16 v[72:75], v[160:163], v[52:55], v[72:75]\n\t"
    "v_cvt_pk_bf16_f32 v66, v68, v69\n\t"
    "v_cvt_pk_bf16_f32 v67, v70, v71\n\t"
    "v_mfma_f32_16x16x32_bf16 v[80:83], v[152:155], v[52:55], v[80:83]\n\t"
    "ds_write_b128 v11, v[64:67] offset:26880\n\t"
    "ds_read_b128 v[40:43], v3 offset:38528\n\t"
    "v_mfma_f32_16x16x32_bf16 v[88:91], v[144:147], v[52:55], v[88:91]\n\t"
    "ds_read_b128 v[68:71], v3 offset:39872\n\t"
    "s_waitcnt lgkmcnt(6)\n\t"
    "v_mfma_f32_16x16x32_bf16 v[76:79], v[164:167], v[48:51], v[76:79]\n\t"
    "ds_read_b128 v[52:55], v3 offset:43456\n\t"
    "v_mfma_f32_16x16x32_bf16 v[84:87], v[156:159], v[48:51], v[84:87]\n\t"
    "ds_read_b128 v[64:67], v3 offset:44800\n\t"
    "v_mfma_f32_16x16x32_bf16 v[92:95], v[148:151], v[48:51], v[92:95]\n\t"
    "v_cvt_pk_bf16_f32 v72, v72, v73\n\t"
    "v_cvt_pk_bf16_f32 v73, v74, v75\n\t"
    "s_waitcnt lgkmcnt(6)\n\t"
    "v_mfma_f32_16x16x32_bf16 v[80:83], v[136:139], v[60:63], v[80:83]\n\t"
    "v_cvt_pk_bf16_f32 v74, v76, v77\n\t"
    "v_cvt_pk_bf16_f32 v75, v78, v79\n\t"
    "v_mfma_f32_16x16x32_bf16 v[88:91], v[128:131], v[60:63], v[88:91]\n\t"
    "ds_write_b128 v11, v[72:75] offset:29568\n\t"
    "ds_read_b128 v[48:51], v3 offset:43904\n\t"
    "s_waitcnt lgkmcnt(7)\n\t"
    "v_mfma_f32_16x16x32_bf16 v[84:87], v[140:143], v[20:23], v[84:87]\n\t"
    "ds_read_b128 v[148:151], v3 offset:45248\n\t"
    "v_mfma_f32_16x16x32_bf16 v[92:95], v[132:135], v[20:23], v[92:95]\n\t"
    "s_waitcnt lgkmcnt(6)\n\t"
    "v_mfma_f32_16x16x32_bf16 v[80:83], v[160:163], v[40:43], v[80:83]\n\t"
    "v_mfma_f32_16x16x32_bf16 v[88:91], v[152:155], v[40:43], v[88:91]\n\t"
    "s_waitcnt lgkmcnt(4)\n\t"
    "v_mfma_f32_16x16x32_bf16 v[84:87], v[164:167], v[68:71], v[84:87]\n\t"
    "v_mfma_f32_16x16x32_bf16 v[92:95], v[156:159], v[68:71], v[92:95]\n\t"
    "v_mfma_f32_16x16x32_bf16 v[88:91], v[136:139], v[52:55], v[88:91]\n\t"
    "s_nop 2\n\t"
    "v_cvt_pk_bf16_f32 v80, v80, v81\n\t"
    "v_cvt_pk_bf16_f32 v81, v82, v83\n\t"
    "s_waitcnt lgkmcnt(3)\n\t"
    "v_mfma_f32_16x16x32_bf16 v[92:95], v[140:143], v[64:67], v[92:95]\n\t"
    "v_cvt_pk_bf16_f32 v82, v84, v85\n\t"
    "v_cvt_pk_bf16_f32 v83, v86, v87\n\t"
    "s_waitcnt lgkmcnt(1)\n\t"
    "v_mfma_f32_16x16x32_bf16 v[88:91], v[160:163], v[48:51], v[88:91]\n\t"
    "ds_write_b128 v11, v[80:83] offset:32256\n\t"
    "s_waitcnt lgkmcnt(1)\n\t"
    "v_mfma_f32_16x16x32_bf16 v[92:95], v[164:167], v[148:151], v[92:95]\n\t"
    "s_nop 4\n\t"
    "v_cvt_pk_bf16_f32 v88, v88, v89\n\t"
    "v_cvt_pk_bf16_f32 v89, v90, v91\n\t"
    "s_nop 0\n\t"
    "v_cvt_pk_bf16_f32 v90, v92, v93\n\t"
    "v_cvt_pk_bf16_f32 v91, v94, v95\n\t"
    "ds_write_b128 v11, v[88:91] offset:34944\n\t"
    "v_mov_b32 v10, 0\n\t"
    "ds_write_b32 v9, v10\n\t"
    "L_finG:\n\t"
    "s_branch L_end\n\t"
    "L_E:\n\t"
    "s_cmp_ge_u32 s37, 3\n\t"
    "s_cselect_b32 s34, 1, 0\n\t"
    "s_cselect_b32 s35, 3, 0\n\t"
    "s_sub_u32 s35, s37, s35\n\t"
    "s_mul_i32 s44, s35, 896\n\t"
    "s_add_u32 s44, s44, 86016\n\t"
    "v_add_u32 v11, s44, v3\n\t"
    "s_mul_i32 s44, s34, 1344\n\t"
    "s_add_u32 s44, s44, 2688\n\t"
    "v_add_u32 v3, s44, v3\n\t"
    "v_cndmask_b32 v3, v19, v3, vcc\n\t"
    "v_cndmask_b32 v11, v19, v11, vcc\n\t"
    "v_and_b32 v5, 1, v16\n\t"
    "s_mul_i32 s44, s34, 12544\n\t"
    "s_lshl_b32 s36, s35, 6\n\t"
    "s_add_u32 s44, s44, s36\n\t"
    "s_add_u32 s44, s44, 86016\n\t"
    "v_mul_u32_u24 v4, 384, v17\n\t"
    "v_lshl_add_u32 v4, v5, 5, v4\n\t"
    "v_lshl_add_u32 v5, v5, 5, v21\n\t"
    "v_lshl_add_u32 v5, v22, 3, v5\n\t"
    "v_add_u32 v5, s44, v5\n\t"
    "v_xor_b32 v6, 32, v5\n\t"
    "s_lshl_b32 s44, s35, 1\n\t"
    "s_lshl_b32 s38, 48, s44\n\t"
    "v_and_b32 v21, 2, v16\n\t"
    "s_mov_b32 s44, 301056\n\t"
    "v_lshl_add_u32 v4, v21, 3, v4\n\t"
    "v_mad_u32_u24 v4, v18, s44, v4\n\t"
    "v_mov_b32 v21, 1073741824\n\t"
    "s_mul_i32 s44, s6, 602112\n\t"
    "s_mul_i32 s39, s33, 2688\n\t"
    "s_add_u32 s39, s39, s44\n\t"
    "v_cndmask_b32 v4, v21, v4, vcc\n\t"
    "s_lshl_b32 s44, s37, 6\n\t"
    "s_add_u32 s39, s39, s44\n\t"
    "s_mov_b32 s26, 9633792\n\t"
    "s_mov_b32 s27, 0x20000\n\t"
    "s_mul_i32 s44, s34, 2688\n\t"
    "v_add_u32 v1, s44, v11\n\t"
    "v_and_b32 v21, 16, v2\n\t"
    "s_lshl_b32 s44, s34, 15\n\t"
    "s_or_b32 s44, s44, 0x3f80\n\t"
    "v_lshlrev_b32 v22, v21, s44\n\t"
    "s_mov_b32 vcc_lo, 0x00300003\n\t"
    "s_mov_b32 vcc_hi, 0x30000300\n\t"
    "v_and_b32 v21, 0x7fff7fff, v22\n\t"
    "v_cndmask_b32 v14, 0, v22, vcc\n\t"
    "v_cndmask_b32 v12, 0, v21, vcc\n\t"
    "s_lshl_b64 vcc, vcc, 2\n\t"
    "v_cndmask_b32 v15, 0, v22, vcc\n\t"
    "v_cndmask_b32 v13, 0, v21, vcc\n\t"
    "s_lshl_b32 s42, 0x1000000, s5\n\t"
    "s_xor_b32 s42, s42, 0xff000000\n\t"
    "s_waitcnt lgkmcnt(0)\n\t"
    "s_barrier\n\t"
    "s_setprio 2\n\t"
    "L_poll59:\n\t"
    "ds_read_b32 v8, v7\n\t"
    "s_waitcnt lgkmcnt(0)\n\t"
    "v_cmp_lt_u32 vcc, 0, v8\n\t"
    "s_and_b32 s44, vcc_lo, 0xf000\n\t"
    "s_cbranch_scc0 L_polld59\n\t"
    "s_sub_u32 s43, s43, 1\n\t"
    "s_cbranch_scc1 L_end\n\t"
    "s_branch L_poll59\n\t"
    "L_polld59:\n\t"
    "ds_read_b64_tr_b16 v[16:17], v123 offset:0\n\t"
    "ds_read_b64_tr_b16 v[20:21], v123 offset:1536\n\t"
    "ds_read_b64_tr_b16 v[18:19], v123 offset:768\n\t"
    "ds_read_b64_tr_b16 v[22:23], v123 offset:2304\n\t"
    "ds_read_b64_tr_b16 v[24:25], v123 offset:6144\n\t"
    "ds_read_b64_tr_b16 v[28:29], v123 offset:7680\n\t"
    "ds_read_b64_tr_b16 v[26:27], v123 offset:6912\n\t"
    "ds_read_b64_tr_b16 v[30:31], v123 offset:8448\n\t"
    "ds_read_b64_tr_b16 v[32:33], v123 offset:12288\n\t"
    "ds_read_b64_tr_b16 v[36:37], v123 offset:13824\n\t"
    "ds_read_b64_tr_b16 v[34:35], v123 offset:13056\n\t"
    "ds_read_b64_tr_b16 v[38:39], v123 offset:14592\n\t"
    "ds_read_b64_tr_b16 v[40:41], v123 offset:64\n\t"
    "ds_read_b64_tr_b16 v[44:45], v123 offset:1600\n\t"
    "ds_read_b64_tr_b16 v[42:43], v123 offset:832\n\t"
    "ds_read_b64_tr_b16 v[46:47], v123 offset:2368\n\t"
    "ds_read_b64_tr_b16 v[48:49], v123 offset:6208\n\t"
    "ds_read_b64_tr_b16 v[52:53], v123 offset:7744\n\t"
    "ds_read_b64_tr_b16 v[50:51], v123 offset:6976\n\t"
    "ds_read_b64_tr_b16 v[54:55], v123 offset:8512\n\t"
    "ds_read_b64_tr_b16 v[56:57], v123 offset:12352\n\t"
    "ds_read_b64_tr_b16 v[60:61], v123 offset:13888\n\t"
    "ds_read_b64_tr_b16 v[58:59], v123 offset:13120\n\t"
    "ds_read_b64_tr_b16 v[62:63], v123 offset:14656\n\t"
    "ds_read_b64_tr_b16 v[64:65], v123 offset:128\n\t"
    "ds_read_b64_tr_b16 v[68:69], v123 offset:1664\n\t"
    "ds_read_b64_tr_b16 v[66:67], v123 offset:896\n\t"
    "ds_read_b64_tr_b16 v[70:71], v123 offset:2432\n\t"
    "ds_read_b64_tr_b16 v[72:73], v123 offset:6272\n\t"
    "ds_read_b64_tr_b16 v[76:77], v123 offset:7808\n\t"
    "ds_read_b64_tr_b16 v[74:75], v123 offset:7040\n\t"
    "ds_read_b64_tr_b16 v[78:79], v123 offset:8576\n\t"
    "ds_read_b64_tr_b16 v[80:81], v123 offset:12416\n\t"
    "ds_read_b64_tr_b16 v[84:85], v123 offset:13952\n\t"
    "ds_read_b64_tr_b16 v[82:83], v123 offset:13184\n\t"
    "ds_read_b64_tr_b16 v[86:87], v123 offset:14720\n\t"
    "s_waitcnt lgkmcnt(15)\n\t"
    "v_permlane16_swap_b32 v16, v20\n\t"
    "v_permlane16_swap_b32 v17, v21\n\t"
    "v_permlane16_swap_b32 v18, v22\n\t"
    "v_permlane16_swap_b32 v19, v23\n\t"
    "v_permlane16_swap_b32 v24, v28\n\t"
    "v_permlane16_swap_b32 v25, v29\n\t"
    "v_permlane16_swap_b32 v26, v30\n\t"
    "v_permlane16_swap_b32 v27, v31\n\t"
    "v_permlane16_swap_b32 v32, v36\n\t"
    "v_permlane16_swap_b32 v33, v37\n\t"
    "v_permlane16_swap_b32 v34, v38\n\t"
    "v_permlane16_swap_b32 v35, v39\n\t"
    "v_permlane16_swap_b32 v40, v44\n\t"
    "v_permlane16_swap_b32 v41, v45\n\t"
    "v_permlane16_swap_b32 v42, v46\n\t"
    "v_permlane16_swap_b32 v43, v47\n\t"
    "v_permlane16_swap_b32 v48, v52\n\t"
    "v_permlane16_swap_b32 v49, v53\n\t"
    "v_permlane16_swap_b32 v50, v54\n\t"
    "v_permlane16_swap_b32 v51, v55\n\t"
    "s_waitcnt lgkmcnt(14)\n\t"
    "v_permlane16_swap_b32 v56, v60\n\t"
    "v_permlane16_swap_b32 v57, v61\n\t"
    "s_waitcnt lgkmcnt(12)\n\t"
    "v_permlane16_swap_b32 v58, v62\n\t"
    "v_permlane16_swap_b32 v59, v63\n\t"
    "s_waitcnt lgkmcnt(10)\n\t"
    "v_permlane16_swap_b32 v64, v68\n\t"
    "v_permlane16_swap_b32 v65, v69\n\t"
    "s_waitcnt lgkmcnt(8)\n\t"
    "v_permlane16_swap_b32 v66, v70\n\t"
    "v_permlane16_swap_b32 v67, v71\n\t"
    "s_waitcnt lgkmcnt(6)\n\t"
    "v_permlane16_swap_b32 v72, v76\n\t"
    "v_permlane16_swap_b32 v73, v77\n\t"
    "s_waitcnt lgkmcnt(4)\n\t"
    "v_permlane16_swap_b32 v74, v78\n\t"
    "v_permlane16_swap_b32 v75, v79\n\t"
    "s_waitcnt lgkmcnt(2)\n\t"
    "v_permlane16_swap_b32 v80, v84\n\t"
    "v_permlane16_swap_b32 v81, v85\n\t"
    "s_waitcnt lgkmcnt(0)\n\t"
    "v_permlane16_swap_b32 v82, v86\n\t"
    "v_permlane16_swap_b32 v83, v87\n\t"
    "ds_read_b32 v8, v7\n\t"
    "ds_read_b128 v[156:159], v124 offset:0\n\t"
    "ds_read_b128 v[164:167], v125 offset:0\n\t"
    "ds_read_b128 v[88:91], v124 offset:64\n\t"
    "ds_read_b128 v[92:95], v125 offset:64\n\t"
    "ds_read_b128 v[112:115], v124 offset:128\n\t"
    "ds_read_b128 v[116:119], v125 offset:128\n\t"
    "s_waitcnt lgkmcnt(6)\n\t"
    "v_cmp_lt_u32 vcc, 0, v8\n\t"
    "s_and_b32 s44, vcc_lo, 0xf0000\n\t"
    "s_cbranch_scc1 L_poll60\n\t"
    "L_polld60:\n\t"
    "v_mov_b32 v10, 9\n\t"
    "ds_write_b32 v9, v10\n\t"
    "s_waitcnt lgkmcnt(6)\n\t"
    "v_lshlrev_b32 v152, 16, v156\n\t"
    "v_and_b32 v153, s45, v156\n\t"
    "v_lshlrev_b32 v154, 16, v157\n\t"
    "v_and_b32 v155, s45, v157\n\t"
    "v_lshlrev_b32 v156, 16, v158\n\t"
    "v_and_b32 v157, s45, v158\n\t"
    "v_lshlrev_b32 v158, 16, v159\n\t"
    "v_and_b32 v159, s45, v159\n\t"
    "s_waitcnt lgkmcnt(5)\n\t"
    "v_lshlrev_b32 v160, 16, v164\n\t"
    "v_and_b32 v161, s45, v164\n\t"
    "v_lshlrev_b32 v162, 16, v165\n\t"
    "v_and_b32 v163, s45, v165\n\t"
    "v_lshlrev_b32 v164, 16, v166\n\t"
    "v_and_b32 v165, s45, v166\n\t"
    "v_lshlrev_b32 v166, 16, v167\n\t"
    "v_and_b32 v167, s45, v167\n\t"
    "ds_read_b32 v8, v7\n\t"
    "ds_read_b64_tr_b16 v[96:97], v5 offset:0\n\t"
    "ds_read_b64_tr_b16 v[98:99], v5 offset:768\n\t"
    "ds_read_b64_tr_b16 v[100:101], v6 offset:0\n\t"
    "ds_read_b64_tr_b16 v[102:103], v6 offset:768\n\t"
    "s_waitcnt lgkmcnt(4)\n\t"
    "v_cmp_lt_u32 vcc, 0, v8\n\t"
    "s_and_b32 s44, vcc_lo, 0xf00000\n\t"
    "s_cbranch_scc1 L_poll61\n\t"
    "L_polld61:\n\t"
    "v_mov_b32 v10, 8\n\t"
    "ds_write_b32 v9, v10\n\t"
    "ds_read_b32 v8, v7 offset:256\n\t"
    "ds_read_b128 v[128:131], v120 offset:192\n\t"
    "ds_read_b128 v[140:143], v121 offset:0\n\t"
    "ds_read_b128 v[132:135], v120 offset:256\n\t"
    "ds_read_b128 v[144:147], v121 offset:64\n\t"
    "ds_read_b128 v[136:139], v120 offset:320\n\t"
    "ds_read_b128 v[148:151], v121 offset:128\n\t"
    "s_waitcnt lgkmcnt(6)\n\t"
    "v_cmp_lt_u32 vcc, 0, v8\n\t"
    "s_and_b32 s44, vcc_lo, s48\n\t"
    "s_cbranch_scc1 L_poll62\n\t"
    "L_polld62:\n\t"
    "s_nop 0\n\t"
    "s_waitcnt lgkmcnt(5)\n\t"
    "v_mfma_f32_16x16x32_bf16 v[152:155], v[16:19], v[128:131], v[152:155]\n\t"
    "v_mfma_f32_16x16x32_bf16 v[156:159], v[20:23], v[128:131], v[156:159]\n\t"
    "s_waitcnt lgkmcnt(4)\n\t"
    "v_mfma_f32_16x16x32_bf16 v[160:163], v[16:19], v[140:143], v[160:163]\n\t"
    "v_mfma_f32_16x16x32_bf16 v[164:167], v[20:23], v[140:143], v[164:167]\n\t"
    "s_waitcnt lgkmcnt(3)\n\t"
    "v_mfma_f32_16x16x32_bf16 v[152:155], v[24:27], v[132:135], v[152:155]\n\t"
    "v_lshlrev_b32 v16, 16, v88\n\t"
    "v_and_b32 v17, s45, v88\n\t"
    "v_lshlrev_b32 v18, 16, v89\n\t"
    "v_mfma_f32_16x16x32_bf16 v[156:159], v[28:31], v[132:135], v[156:159]\n\t"
    "v_and_b32 v19, s45, v89\n\t"
    "v_lshlrev_b32 v20, 16, v90\n\t"
    "v_and_b32 v21, s45, v90\n\t"
    "s_waitcnt lgkmcnt(2)\n\t"
    "v_mfma_f32_16x16x32_bf16 v[160:163], v[24:27], v[144:147], v[160:163]\n\t"
    "v_lshlrev_b32 v22, 16, v91\n\t"
    "v_and_b32 v23, s45, v91\n\t"
    "v_mfma_f32_16x16x32_bf16 v[164:167], v[28:31], v[144:147], v[164:167]\n\t"
    "s_waitcnt lgkmcnt(0)\n\t"
    "v_mfma_f32_16x16x32_bf16 v[152:155], v[32:35], v[136:139], v[152:155]\n\t"
    "v_lshlrev_b32 v24, 16, v92\n\t"
    "v_and_b32 v25, s45, v92\n\t"
    "v_lshlrev_b32 v26, 16, v93\n\t"
    "v_mfma_f32_16x16x32_bf16 v[156:159], v[36:39], v[136:139], v[156:159]\n\t"
    "v_and_b32 v27, s45, v93\n\t"
    "v_lshlrev_b32 v28, 16, v94\n\t"
    "v_and_b32 v29, s45, v94\n\t"
    "v_mfma_f32_16x16x32_bf16 v[160:163], v[32:35], v[148:151], v[160:163]\n\t"
    "v_lshlrev_b32 v30, 16, v95\n\t"
    "v_and_b32 v31, s45, v95\n\t"
    "v_mfma_f32_16x16x32_bf16 v[164:167], v[36:39], v[148:151], v[164:167]\n\t"
    "v_mfma_f32_16x16x32_bf16 v[16:19], v[40:43], v[128:131], v[16:19]\n\t"
    "s_nop 3\n\t"
    "v_add_f32 v92, v160, v152\n\t"
    "v_add_f32 v93, v161, v153\n\t"
    "v_cvt_pk_bf16_f32 v88, v92, v93\n\t"
    "v_add_f32 v94, v162, v154\n\t"
    "v_mfma_f32_16x16x32_bf16 v[20:23], v[44:47], v[128:131], v[20:23]\n\t"
    "v_add_f32 v95, v163, v155\n\t"
    "v_cvt_pk_bf16_f32 v89, v94, v95\n\t"
    "v_add_f32 v92, v164, v156\n\t"
    "v_add_f32 v93, v165, v157\n\t"
    "v_mfma_f32_16x16x32_bf16 v[24:27], v[40:43], v[140:143], v[24:27]\n\t"
    "v_cvt_pk_bf16_f32 v90, v92, v93\n\t"
    "v_add_f32 v94, v166, v158\n\t"
    "v_add_f32 v95, v167, v159\n\t"
    "v_cvt_pk_bf16_f32 v91, v94, v95\n\t"
    "v_mfma_f32_16x16x32_bf16 v[28:31], v[44:47], v[140:143], v[28:31]\n\t"
    "ds_write_b128 v0, v[88:91] offset:0\n\t"
    "v_lshlrev_b32 v32, 16, v112\n\t"
    "v_and_b32 v33, s45, v112\n\t"
    "v_mfma_f32_16x16x32_bf16 v[16:19], v[48:51], v[132:135], v[16:19]\n\t"
    "v_lshlrev_b32 v34, 16, v113\n\t"
    "v_and_b32 v35, s45, v113\n\t"
    "v_lshlrev_b32 v36, 16, v114\n\t"
    "v_and_b32 v37, s45, v114\n\t"
    "v_mfma_f32_16x16x32_bf16 v[20:23], v[52:55], v[132:135], v[20:23]\n\t"
    "v_lshlrev_b32 v38, 16, v115\n\t"
    "v_and_b32 v39, s45, v115\n\t"
    "v_lshlrev_b32 v40, 16, v116\n\t"
    "v_and_b32 v41, s45, v116\n\t"
    "v_mfma_f32_16x16x32_bf16 v[24:27], v[48:51], v[144:147], v[24:27]\n\t"
    "v_lshlrev_b32 v42, 16, v117\n\t"
    "v_and_b32 v43, s45, v117\n\t"
    "v_lshlrev_b32 v44, 16, v118\n\t"
    "v_and_b32 v45, s45, v118\n\t"
    "v_mfma_f32_16x16x32_bf16 v[28:31], v[52:55], v[144:147], v[28:31]\n\t"
    "v_lshlrev_b32 v46, 16, v119\n\t"
    "v_and_b32 v47, s45, v119\n\t"
    "v_sub_f32 v93, v160, v152\n\t"
    "v_sub_f32 v94, v161, v153\n\t"
    "v_mfma_f32_16x16x32_bf16 v[16:19], v[56:59], v[136:139], v[16:19]\n\t"
    "v_cvt_pk_bf16_f32 v92, v93, v94\n\t"
    "v_sub_f32 v94, v162, v154\n\t"
    "v_sub_f32 v95, v163, v155\n\t"
    "v_cvt_pk_bf16_f32 v93, v94, v95\n\t"
    "v_mfma_f32_16x16x32_bf16 v[20:23], v[60:63], v[136:139], v[20:23]\n\t"
    "v_sub_f32 v94, v164, v156\n\t"
    "v_sub_f32 v95, v165, v157\n\t"
    "v_cvt_pk_bf16_f32 v94, v94, v95\n\t"
    "v_sub_f32 v95, v166, v158\n\t"
    "v_mfma_f32_16x16x32_bf16 v[24:27], v[56:59], v[148:151], v[24:27]\n\t"
    "v_sub_f32 v10, v167, v159\n\t"
    "v_cvt_pk_bf16_f32 v95, v95, v10\n\t"
    "ds_write_b128 v0, v[92:95] offset:1344\n\t"
    "v_mfma_f32_16x16x32_bf16 v[28:31], v[60:63], v[148:151], v[28:31]\n\t"
    "v_mov_b32 v10, 3\n\t"
    "ds_write_b32 v9, v10 offset:208\n\t"
    "v_mfma_f32_16x16x32_bf16 v[32:35], v[64:67], v[128:131], v[32:35]\n\t"
    "s_nop 0\n\t"
    "v_add_f32 v92, v24, v16\n\t"
    "v_add_f32 v93, v25, v17\n\t"
    "v_cvt_pk_bf16_f32 v88, v92, v93\n\t"
    "v_add_f32 v94, v26, v18\n\t"
    "v_mfma_f32_16x16x32_bf16 v[36:39], v[68:71], v[128:131], v[36:39]\n\t"
    "v_add_f32 v95, v27, v19\n\t"
    "v_cvt_pk_bf16_f32 v89, v94, v95\n\t"
    "v_add_f32 v92, v28, v20\n\t"
    "v_add_f32 v93, v29, v21\n\t"
    "v_mfma_f32_16x16x32_bf16 v[40:43], v[64:67], v[140:143], v[40:43]\n\t"
    "v_cvt_pk_bf16_f32 v90, v92, v93\n\t"
    "v_add_f32 v94, v30, v22\n\t"
    "v_add_f32 v95, v31, v23\n\t"
    "v_cvt_pk_bf16_f32 v91, v94, v95\n\t"
    "v_mfma_f32_16x16x32_bf16 v[44:47], v[68:71], v[140:143], v[44:47]\n\t"
    "ds_write_b128 v0, v[88:91] offset:448\n\t"
    "v_sub_f32 v93, v24, v16\n\t"
    "v_sub_f32 v94, v25, v17\n\t"
    "v_mfma_f32_16x16x32_bf16 v[32:35], v[72:75], v[132:135], v[32:35]\n\t"
    "v_cvt_pk_bf16_f32 v92, v93, v94\n\t"
    "v_sub_f32 v94, v26, v18\n\t"
    "v_sub_f32 v95, v27, v19\n\t"
    "v_cvt_pk_bf16_f32 v93, v94, v95\n\t"
    "v_mfma_f32_16x16x32_bf16 v[36:39], v[76:79], v[132:135], v[36:39]\n\t"
    "v_sub_f32 v94, v28, v20\n\t"
    "v_sub_f32 v95, v29, v21\n\t"
    "v_cvt_pk_bf16_f32 v94, v94, v95\n\t"
    "v_sub_f32 v95, v30, v22\n\t"
    "v_mfma_f32_16x16x32_bf16 v[40:43], v[72:75], v[144:147], v[40:43]\n\t"
    "v_sub_f32 v10, v31, v23\n\t"
    "v_cvt_pk_bf16_f32 v95, v95, v10\n\t"
    "ds_write_b128 v0, v[92:95] offset:1792\n\t"
    "v_mfma_f32_16x16x32_bf16 v[44:47], v[76:79], v[144:147], v[44:47]\n\t"
    "v_mfma_f32_16x16x32_bf16 v[32:35], v[80:83], v[136:139], v[32:35]\n\t"
    "v_mfma_f32_16x16x32_bf16 v[36:39], v[84:87], v[136:139], v[36:39]\n\t"
    "v_mfma_f32_16x16x32_bf16 v[40:43], v[80:83], v[148:151], v[40:43]\n\t"
    "v_mfma_f32_16x16x32_bf16 v[44:47], v[84:87], v[148:151], v[44:47]\n\t"
    "s_nop 6\n\t"
    "v_add_f32 v92, v40, v32\n\t"
    "v_add_f32 v93, v41, v33\n\t"
    "v_cvt_pk_bf16_f32 v88, v92, v93\n\t"
    "v_add_f32 v94, v42, v34\n\t"
    "v_add_f32 v95, v43, v35\n\t"
    "v_cvt_pk_bf16_f32 v89, v94, v95\n\t"
    "v_add_f32 v92, v44, v36\n\t"
    "v_add_f32 v93, v45, v37\n\t"
    "v_cvt_pk_bf16_f32 v90, v92, v93\n\t"
    "v_add_f32 v94, v46, v38\n\t"
    "v_add_f32 v95, v47, v39\n\t"
    "v_cvt_pk_bf16_f32 v91, v94, v95\n\t"
    "ds_write_b128 v0, v[88:91] offset:896\n\t"
    "v_sub_f32 v93, v40, v32\n\t"
    "v_sub_f32 v94, v41, v33\n\t"
    "v_cvt_pk_bf16_f32 v92, v93, v94\n\t"
    "v_sub_f32 v94, v42, v34\n\t"
    "v_sub_f32 v95, v43, v35\n\t"
    "v_cvt_pk_bf16_f32 v93, v94, v95\n\t"
    "v_sub_f32 v94, v44, v36\n\t"
    "v_sub_f32 v95, v45, v37\n\t"
    "v_cvt_pk_bf16_f32 v94, v94, v95\n\t"
    "v_sub_f32 v95, v46, v38\n\t"
    "v_sub_f32 v10, v47, v39\n\t"
    "v_cvt_pk_bf16_f32 v95, v95, v10\n\t"
    "ds_write_b128 v0, v[92:95] offset:2240\n\t"
    "v_mov_b32 v10, 1\n\t"
    "ds_write_b32 v9, v10 offset:208\n\t"
    "ds_read_b32 v8, v7\n\t"
    "v_sub_f32_dpp v40, v40, v40 row_ror:1 row_mask:0xf bank_mask:0xf bound_ctrl:0\n\t"
    "v_sub_f32_dpp v41, v41, v41 row_ror:1 row_mask:0xf bank_mask:0xf bound_ctrl:0\n\t"
    "v_sub_f32_dpp v42, v42, v42 row_ror:1 row_mask:0xf bank_mask:0xf bound_ctrl:0\n\t"
    "v_sub_f32_dpp v43, v43, v43 row_ror:1 row_mask:0xf bank_mask:0xf bound_ctrl:0\n\t"
    "v_sub_f32_dpp v44, v44, v44 row_ror:1 row_mask:0xf bank_mask:0xf bound_ctrl:0\n\t"
    "v_sub_f32_dpp v45, v45, v45 row_ror:1 row_mask:0xf bank_mask:0xf bound_ctrl:0\n\t"
    "v_sub_f32_dpp v46, v46, v46 row_ror:1 row_mask:0xf bank_mask:0xf bound_ctrl:0\n\t"
    "v_sub_f32_dpp v47, v47, v47 row_ror:1 row_mask:0xf bank_mask:0xf bound_ctrl:0\n\t"
    "v_cvt_pk_bf16_f32 v40, v40, v41\n\t"
    "v_cvt_pk_bf16_f32 v41, v42, v43\n\t"
    "v_cvt_pk_bf16_f32 v42, v44, v45\n\t"
    "v_cvt_pk_bf16_f32 v43, v46, v47\n\t"
    "s_waitcnt lgkmcnt(0)\n\t"
    "v_cmp_lt_u32 vcc, 0, v8\n\t"
    "s_and_b32 s44, vcc_lo, 0xf000000\n\t"
    "s_cbranch_scc1 L_slow63\n\t"
    "L_cont63:\n\t"
    "ds_read_b64_tr_b16 v[104:105], v5 offset:18816\n\t"
    "ds_read_b64_tr_b16 v[106:107], v5 offset:19584\n\t"
    "ds_read_b64_tr_b16 v[108:109], v6 offset:18816\n\t"
    "ds_read_b64_tr_b16 v[110:111], v6 offset:19584\n\t"
    "v_mov_b32 v10, 7\n\t"
    "ds_write_b32 v9, v10\n\t"
    "ds_write_b128 v0, v[40:43] offset:3584\n\t"
    "v_sub_f32_dpp v32, v32, v32 row_shl:1 row_mask:0xf bank_mask:0xf bound_ctrl:0\n\t"
    "v_sub_f32_dpp v33, v33, v33 row_shl:1 row_mask:0xf bank_mask:0xf bound_ctrl:0\n\t"
    "v_sub_f32_dpp v34, v34, v34 row_shl:1 row_mask:0xf bank_mask:0xf bound_ctrl:0\n\t"
    "v_sub_f32_dpp v35, v35, v35 row_shl:1 row_mask:0xf bank_mask:0xf bound_ctrl:0\n\t"
    "v_sub_f32_dpp v36, v36, v36 row_shl:1 row_mask:0xf bank_mask:0xf bound_ctrl:0\n\t"
    "v_sub_f32_dpp v37, v37, v37 row_shl:1 row_mask:0xf bank_mask:0xf bound_ctrl:0\n\t"
    "v_sub_f32_dpp v38, v38, v38 row_shl:1 row_mask:0xf bank_mask:0xf bound_ctrl:0\n\t"
    "v_sub_f32_dpp v39, v39, v39 row_shl:1 row_mask:0xf bank_mask:0xf bound_ctrl:0\n\t"
    "v_cvt_pk_bf16_f32 v32, v32, v33\n\t"
    "v_cvt_pk_bf16_f32 v33, v34, v35\n\t"
    "v_cvt_pk_bf16_f32 v34, v36, v37\n\t"
    "v_cvt_pk_bf16_f32 v35, v38, v39\n\t"
    "ds_write_b128 v0, v[32:35] offset:4928\n\t"
    "v_sub_f32_dpp v24, v24, v24 row_ror:1 row_mask:0xf bank_mask:0xf bound_ctrl:0\n\t"
    "v_sub_f32_dpp v25, v25, v25 row_ror:1 row_mask:0xf bank_mask:0xf bound_ctrl:0\n\t"
    "v_sub_f32_dpp v26, v26, v26 row_ror:1 row_mask:0xf bank_mask:0xf bound_ctrl:0\n\t"
    "v_sub_f32_dpp v27, v27, v27 row_ror:1 row_mask:0xf bank_mask:0xf bound_ctrl:0\n\t"
    "v_sub_f32_dpp v28, v28, v28 row_ror:1 row_mask:0xf bank_mask:0xf bound_ctrl:0\n\t"
    "v_sub_f32_dpp v29, v29, v29 row_ror:1 row_mask:0xf bank_mask:0xf bound_ctrl:0\n\t"
    "v_sub_f32_dpp v30, v30, v30 row_ror:1 row_mask:0xf bank_mask:0xf bound_ctrl:0\n\t"
    "v_sub_f32_dpp v31, v31, v31 row_ror:1 row_mask:0xf bank_mask:0xf bound_ctrl:0\n\t"
    "v_cvt_pk_bf16_f32 v24, v24, v25\n\t"
    "v_cvt_pk_bf16_f32 v25, v26, v27\n\t"
    "v_cvt_pk_bf16_f32 v26, v28, v29\n\t"
    "v_cvt_pk_bf16_f32 v27, v30, v31\n\t"
    "ds_write_b128 v0, v[24:27] offset:3136\n\t"
    "v_sub_f32_dpp v16, v16, v16 row_shl:1 row_mask:0xf bank_mask:0xf bound_ctrl:0\n\t"
    "v_sub_f32_dpp v17, v17, v17 row_shl:1 row_mask:0xf bank_mask:0xf bound_ctrl:0\n\t"
    "v_sub_f32_dpp v18, v18, v18 row_shl:1 row_mask:0xf bank_mask:0xf bound_ctrl:0\n\t"
    "v_sub_f32_dpp v19, v19, v19 row_shl:1 row_mask:0xf bank_mask:0xf bound_ctrl:0\n\t"
    "v_sub_f32_dpp v20, v20, v20 row_shl:1 row_mask:0xf bank_mask:0xf bound_ctrl:0\n\t"
    "v_sub_f32_dpp v21, v21, v21 row_shl:1 row_mask:0xf bank_mask:0xf bound_ctrl:0\n\t"
    "v_sub_f32_dpp v22, v22, v22 row_shl:1 row_mask:0xf bank_mask:0xf bound_ctrl:0\n\t"
    "v_sub_f32_dpp v23, v23, v23 row_shl:1 row_mask:0xf bank_mask:0xf bound_ctrl:0\n\t"
    "v_cvt_pk_bf16_f32 v16, v16, v17\n\t"
    "v_cvt_pk_bf16_f32 v17, v18, v19\n\t"
    "v_cvt_pk_bf16_f32 v18, v20, v21\n\t"
    "v_cvt_pk_bf16_f32 v19, v22, v23\n\t"
    "ds_write_b128 v0, v[16:19] offset:4480\n\t"
    "v_sub_f32_dpp v160, v160, v160 row_ror:1 row_mask:0xf bank_mask:0xf bound_ctrl:0\n\t"
    "v_sub_f32_dpp v161, v161, v161 row_ror:1 row_mask:0xf bank_mask:0xf bound_ctrl:0\n\t"
    "v_sub_f32_dpp v162, v162, v162 row_ror:1 row_mask:0xf bank_mask:0xf bound_ctrl:0\n\t"
    "v_sub_f32_dpp v163, v163, v163 row_ror:1 row_mask:0xf bank_mask:0xf bound_ctrl:0\n\t"
    "v_sub_f32_dpp v164, v164, v164 row_ror:1 row_mask:0xf bank_mask:0xf bound_ctrl:0\n\t"
    "v_sub_f32_dpp v165, v165, v165 row_ror:1 row_mask:0xf bank_mask:0xf bound_ctrl:0\n\t"
    "v_sub_f32_dpp v166, v166, v166 row_ror:1 row_mask:0xf bank_mask:0xf bound_ctrl:0\n\t"
    "v_sub_f32_dpp v167, v167, v167 row_ror:1 row_mask:0xf bank_mask:0xf bound_ctrl:0\n\t"
    "v_cvt_pk_bf16_f32 v160, v160, v161\n\t"
    "v_cvt_pk_bf16_f32 v161, v162, v163\n\t"
    "v_cvt_pk_bf16_f32 v162, v164, v165\n\t"
    "v_cvt_pk_bf16_f32 v163, v166, v167\n\t"
    "ds_write_b128 v0, v[160:163] offset:2688\n\t"
    "ds_read_b32 v8, v7\n\t"
    "v_sub_f32_dpp v152, v152, v152 row_shl:1 row_mask:0xf bank_mask:0xf bound_ctrl:0\n\t"
    "v_sub_f32_dpp v153, v153, v153 row_shl:1 row_mask:0xf bank_mask:0xf bound_ctrl:0\n\t"
    "v_sub_f32_dpp v154, v154, v154 row_shl:1 row_mask:0xf bank_mask:0xf bound_ctrl:0\n\t"
    "v_sub_f32_dpp v155, v155, v155 row_shl:1 row_mask:0xf bank_mask:0xf bound_ctrl:0\n\t"
    "v_sub_f32_dpp v156, v156, v156 row_shl:1 row_mask:0xf bank_mask:0xf bound_ctrl:0\n\t"
    "v_sub_f32_dpp v157, v157, v157 row_shl:1 row_mask:0xf bank_mask:0xf bound_ctrl:0\n\t"
    "v_sub_f32_dpp v158, v158, v158 row_shl:1 row_mask:0xf bank_mask:0xf bound_ctrl:0\n\t"
    "v_sub_f32_dpp v159, v159, v159 row_shl:1 row_mask:0xf bank_mask:0xf bound_ctrl:0\n\t"
    "v_cvt_pk_bf16_f32 v152, v152, v153\n\t"
    "v_cvt_pk_bf16_f32 v153, v154, v155\n\t"
    "v_cvt_pk_bf16_f32 v154, v156, v157\n\t"
    "v_cvt_pk_bf16_f32 v155, v158, v159\n\t"
    "ds_write_b128 v0, v[152:155] offset:4032\n\t"
    "v_mov_b32 v10, 0\n\t"
    "ds_write_b32 v9, v10 offset:208\n\t"
    "s_setprio 3\n\t"
    "s_waitcnt lgkmcnt(2)\n\t"
    "v_cmp_lt_u32 vcc, 0, v8\n\t"
    "s_and_b32 s44, vcc_lo, 0xf0000000\n\t"
    "s_cbranch_scc1 L_slow64\n\t"
    "L_cont64:\n\t"
    "ds_read_b64_tr_b16 v[112:113], v5 offset:37632\n\t"
    "ds_read_b64_tr_b16 v[114:115], v5 offset:38400\n\t"
    "ds_read_b64_tr_b16 v[116:117], v6 offset:37632\n\t"
    "ds_read_b64_tr_b16 v[118:119], v6 offset:38400\n\t"
    "v_mov_b32 v10, 6\n\t"
    "ds_write_b32 v9, v10\n\t"
    "v_cmp_lt_u32 vcc, 0, v8\n\t"
    "s_and_b32 s44, vcc_hi, 0xf\n\t"
    "s_cbranch_scc1 L_slow65\n\t"
    "L_cont65:\n\t"
    "ds_read_b64_tr_b16 v[120:121], v5 offset:56448\n\t"
    "ds_read_b64_tr_b16 v[122:123], v5 offset:57216\n\t"
    "ds_read_b64_tr_b16 v[124:125], v6 offset:56448\n\t"
    "ds_read_b64_tr_b16 v[126:127], v6 offset:57216\n\t"
    "v_cmp_lt_u32 vcc, 0, v8\n\t"
    "v_mov_b32 v10, 5\n\t"
    "ds_write_b32 v9, v10\n\t"
    "s_and_b32 s44, vcc_hi, s42\n\t"
    "s_cbranch_scc1 L_slow66\n\t"
    "L_cont66:\n\t"
    "ds_read_b128 v[128:131], v3 offset:0\n\t"
    "ds_read_b128 v[132:135], v3 offset:448\n\t"
    "ds_read_b128 v[56:59], v3 offset:5376\n\t"
    "ds_read_b32 v8, v7\n\t"
    "ds_read_b128 v[136:139], v3 offset:10752\n\t"
    "s_waitcnt lgkmcnt(4)\n\t"
    "v_mfma_f32_16x16x32_bf16 v[40:43], v[96:99], v[128:131], 0\n\t"
    "v_mfma_f32_16x16x32_bf16 v[44:47], v[100:103], v[128:131], 0\n\t"
    "s_waitcnt lgkmcnt(3)\n\t"
    "v_mfma_f32_16x16x32_bf16 v[40:43], v[120:123], v[132:135], v[40:43]\n\t"
    "s_waitcnt lgkmcnt(1)\n\t"
    "v_cmp_lt_u32 vcc, 0, v8\n\t"
    "v_mfma_f32_16x16x32_bf16 v[44:47], v[124:127], v[132:135], v[44:47]\n\t"
    "s_and_b32 s44, vcc_hi, 0xf0\n\t"
    "s_cbranch_scc1 L_slow67\n\t"
    "L_cont67:\n\t"
    "ds_read_b64_tr_b16 v[128:129], v5 offset:0\n\t"
    "v_mfma_f32_16x16x32_bf16 v[40:43], v[104:107], v[56:59], v[40:43]\n\t"
    "ds_read_b64_tr_b16 v[130:131], v5 offset:768\n\t"
    "ds_read_b64_tr_b16 v[132:133], v6 offset:0\n\t"
    "ds_read_b64_tr_b16 v[134:135], v6 offset:768\n\t"
    "v_mfma_f32_16x16x32_bf16 v[44:47], v[108:111], v[56:59], v[44:47]\n\t"
    "v_mov_b32 v10, 4\n\t"
    "ds_write_b32 v9, v10\n\t"
    "ds_read_b128 v[140:143], v3 offset:5824\n\t"
    "v_mfma_f32_16x16x32_bf16 v[48:51], v[96:99], v[56:59], 0\n\t"
    "v_mfma_f32_16x16x32_bf16 v[52:55], v[100:103], v[56:59], 0\n\t"
    "s_waitcnt lgkmcnt(6)\n\t"
    "v_mfma_f32_16x16x32_bf16 v[40:43], v[112:115], v[136:139], v[40:43]\n\t"
    "v_mfma_f32_16x16x32_bf16 v[44:47], v[116:119], v[136:139], v[44:47]\n\t"
    "ds_read_b128 v[72:75], v3 offset:16128\n\t"
    "v_mfma_f32_16x16x32_bf16 v[48:51], v[104:107], v[136:139], v[48:51]\n\t"
    "v_mfma_f32_16x16x32_bf16 v[52:55], v[108:111], v[136:139], v[52:55]\n\t"
    "ds_read_b32 v8, v7\n\t"
    "v_mfma_f32_16x16x32_bf16 v[56:59], v[96:99], v[136:139], 0\n\t"
    "v_mfma_f32_16x16x32_bf16 v[60:63], v[100:103], v[136:139], 0\n\t"
    "s_waitcnt lgkmcnt(2)\n\t"
    "v_mfma_f32_16x16x32_bf16 v[40:43], v[128:131], v[140:143], v[40:43]\n\t"
    "v_mfma_f32_16x16x32_bf16 v[44:47], v[132:135], v[140:143], v[44:47]\n\t"
    "ds_read_b128 v[144:147], v3 offset:21504\n\t"
    "v_mfma_f32_16x16x32_bf16 v[48:51], v[120:123], v[140:143], v[48:51]\n\t"
    "s_waitcnt lgkmcnt(1)\n\t"
    "v_cmp_lt_u32 vcc, 0, v8\n\t"
    "v_mfma_f32_16x16x32_bf16 v[52:55], v[124:127], v[140:143], v[52:55]\n\t"
    "s_and_b32 s44, vcc_hi, 0xf00\n\t"
    "s_cbranch_scc1 L_slow68\n\t"
    "L_cont68:\n\t"
    "ds_read_b64_tr_b16 v[136:137], v5 offset:18816\n\t"
    "v_mfma_f32_16x16x32_bf16 v[48:51], v[112:115], v[72:75], v[48:51]\n\t"
    "ds_read_b64_tr_b16 v[138:139], v5 offset:19584\n\t"
    "ds_read_b64_tr_b16 v[140:141], v6 offset:18816\n\t"
    "ds_read_b64_tr_b16 v[142:143], v6 offset:19584\n\t"
    "v_mfma_f32_16x16x32_bf16 v[52:55], v[116:119], v[72:75], v[52:55]\n\t"
    "v_mfma_f32_16x16x32_bf16 v[56:59], v[104:107], v[72:75], v[56:59]\n\t"
    "v_mfma_f32_16x16x32_bf16 v[60:63], v[108:111], v[72:75], v[60:63]\n\t"
    "ds_read_b128 v[152:155], v3 offset:11200\n\t"
    "v_mfma_f32_16x16x32_bf16 v[64:67], v[96:99], v[72:75], 0\n\t"
    "v_mfma_f32_16x16x32_bf16 v[68:71], v[100:103], v[72:75], 0\n\t"
    "s_waitcnt lgkmcnt(5)\n\t"
    "v_mfma_f32_16x16x32_bf16 v[56:59], v[112:115], v[144:147], v[56:59]\n\t"
    "v_mfma_f32_16x16x32_bf16 v[60:63], v[116:119], v[144:147], v[60:63]\n\t"
    "v_mfma_f32_16x16x32_bf16 v[64:67], v[104:107], v[144:147], v[64:67]\n\t"
    "v_mfma_f32_16x16x32_bf16 v[68:71], v[108:111], v[144:147], v[68:71]\n\t"
    "ds_read_b128 v[156:159], v3 offset:16576\n\t"
    "ds_read_b32 v8, v7\n\t"
    "v_mfma_f32_16x16x32_bf16 v[72:75], v[96:99], v[144:147], 0\n\t"
    "v_mfma_f32_16x16x32_bf16 v[76:79], v[100:103], v[144:147], 0\n\t"
    "s_waitcnt lgkmcnt(2)\n\t"
    "v_mfma_f32_16x16x32_bf16 v[40:43], v[136:139], v[152:155], v[40:43]\n\t"
    "v_mfma_f32_16x16x32_bf16 v[44:47], v[140:143], v[152:155], v[44:47]\n\t"
    "v_mfma_f32_16x16x32_bf16 v[48:51], v[128:131], v[152:155], v[48:51]\n\t"
    "ds_read_b128 v[80:83], v3 offset:21952\n\t"
    "s_waitcnt lgkmcnt(1)\n\t"
    "v_cmp_lt_u32 vcc, 0, v8\n\t"
    "v_mfma_f32_16x16x32_bf16 v[52:55], v[132:135], v[152:155], v[52:55]\n\t"
    "s_and_b32 s44, vcc_hi, 0xf000\n\t"
    "s_cbranch_scc1 L_slow69\n\t"
    "L_cont69:\n\t"
    "ds_read_b64_tr_b16 v[144:145], v5 offset:37632\n\t"
    "v_mfma_f32_16x16x32_bf16 v[56:59], v[120:123], v[152:155], v[56:59]\n\t"
    "ds_read_b64_tr_b16 v[146:147], v5 offset:38400\n\t"
    "ds_read_b64_tr_b16 v[148:149], v6 offset:37632\n\t"
    "ds_read_b64_tr_b16 v[150:151], v6 offset:38400\n\t"
    "v_mfma_f32_16x16x32_bf16 v[60:63], v[124:127], v[152:155], v[60:63]\n\t"
    "v_mfma_f32_16x16x32_bf16 v[48:51], v[136:139], v[156:159], v[48:51]\n\t"
    "v_mfma_f32_16x16x32_bf16 v[52:55], v[140:143], v[156:159], v[52:55]\n\t"
    "v_mfma_f32_16x16x32_bf16 v[56:59], v[128:131], v[156:159], v[56:59]\n\t"
    "v_mfma_f32_16x16x32_bf16 v[60:63], v[132:135], v[156:159], v[60:63]\n\t"
    "ds_read_b128 v[84:87], v3 offset:896\n\t"
    "v_mfma_f32_16x16x32_bf16 v[64:67], v[120:123], v[156:159], v[64:67]\n\t"
    "v_mfma_f32_16x16x32_bf16 v[68:71], v[124:127], v[156:159], v[68:71]\n\t"
    "ds_read_b128 v[160:163], v3 offset:26880\n\t"
    "ds_read_b32 v8, v7\n\t"
    "s_setprio 2\n\t"
    "s_waitcnt lgkmcnt(6)\n\t"
    "v_mfma_f32_16x16x32_bf16 v[56:59], v[136:139], v[80:83], v[56:59]\n\t"
    "v_mfma_f32_16x16x32_bf16 v[60:63], v[140:143], v[80:83], v[60:63]\n\t"
    "v_mfma_f32_16x16x32_bf16 v[64:67], v[128:131], v[80:83], v[64:67]\n\t"
    "v_mfma_f32_16x16x32_bf16 v[68:71], v[132:135], v[80:83], v[68:71]\n\t"
    "v_mfma_f32_16x16x32_bf16 v[72:75], v[120:123], v[80:83], v[72:75]\n\t"
    "ds_read_b128 v[164:167], v3 offset:27328\n\t"
    "s_waitcnt lgkmcnt(1)\n\t"
    "v_cmp_lt_u32 vcc, 0, v8\n\t"
    "v_mfma_f32_16x16x32_bf16 v[76:79], v[124:127], v[80:83], v[76:79]\n\t"
    "s_and_b32 s44, vcc_hi, 0xf0000\n\t"
    "s_cbranch_scc1 L_slow70\n\t"
    "L_cont70:\n\t"
    "ds_read_b64_tr_b16 v[152:153], v5 offset:56448\n\t"
    "v_mfma_f32_16x16x32_bf16 v[40:43], v[144:147], v[84:87], v[40:43]\n\t"
    "ds_read_b64_tr_b16 v[154:155], v5 offset:57216\n\t"
    "ds_read_b64_tr_b16 v[156:157], v6 offset:56448\n\t"
    "ds_read_b64_tr_b16 v[158:159], v6 offset:57216\n\t"
    "v_mfma_f32_16x16x32_bf16 v[44:47], v[148:151], v[84:87], v[44:47]\n\t"
    "v_mov_b32 v10, 3\n\t"
    "ds_write_b32 v9, v10\n\t"
    "v_mfma_f32_16x16x32_bf16 v[64:67], v[112:115], v[160:163], v[64:67]\n\t"
    "v_mfma_f32_16x16x32_bf16 v[68:71], v[116:119], v[160:163], v[68:71]\n\t"
    "v_mfma_f32_16x16x32_bf16 v[72:75], v[104:107], v[160:163], v[72:75]\n\t"
    "v_mfma_f32_16x16x32_bf16 v[76:79], v[108:111], v[160:163], v[76:79]\n\t"
    "ds_read_b128 v[88:91], v3 offset:6272\n\t"
    "v_mfma_f32_16x16x32_bf16 v[80:83], v[96:99], v[160:163], 0\n\t"
    "v_mfma_f32_16x16x32_bf16 v[84:87], v[100:103], v[160:163], 0\n\t"
    "ds_read_b32 v8, v7\n\t"
    "s_waitcnt lgkmcnt(6)\n\t"
    "v_mfma_f32_16x16x32_bf16 v[64:67], v[136:139], v[164:167], v[64:67]\n\t"
    "v_mfma_f32_16x16x32_bf16 v[68:71], v[140:143], v[164:167], v[68:71]\n\t"
    "ds_read_b128 v[16:19], v3 offset:32256\n\t"
    "v_mfma_f32_16x16x32_bf16 v[72:75], v[128:131], v[164:167], v[72:75]\n\t"
    "v_mfma_f32_16x16x32_bf16 v[76:79], v[132:135], v[164:167], v[76:79]\n\t"
    "v_mfma_f32_16x16x32_bf16 v[80:83], v[120:123], v[164:167], v[80:83]\n\t"
    "s_waitcnt lgkmcnt(1)\n\t"
    "v_cmp_lt_u32 vcc, 0, v8\n\t"
    "v_mfma_f32_16x16x32_bf16 v[84:87], v[124:127], v[164:167], v[84:87]\n\t"
    "s_and_b32 s44, vcc_hi, 0xf00000\n\t"
    "s_cbranch_scc1 L_slow71\n\t"
    "L_cont71:\n\t"
    "ds_read_b64_tr_b16 v[160:161], v5 offset:0\n\t"
    "v_mfma_f32_16x16x32_bf16 v[40:43], v[152:155], v[88:91], v[40:43]\n\t"
    "ds_read_b64_tr_b16 v[162:163], v5 offset:768\n\t"
    "ds_read_b64_tr_b16 v[164:165], v6 offset:0\n\t"
    "ds_read_b64_tr_b16 v[166:167], v6 offset:768\n\t"
    "v_mfma_f32_16x16x32_bf16 v[44:47], v[156:159], v[88:91], v[44:47]\n\t"
    "ds_read_b128 v[20:23], v3 offset:11648\n\t"
    "v_mfma_f32_16x16x32_bf16 v[48:51], v[144:147], v[88:91], v[48:51]\n\t"
    "v_mfma_f32_16x16x32_bf16 v[52:55], v[148:151], v[88:91], v[52:55]\n\t"
    "s_waitcnt lgkmcnt(5)\n\t"
    "v_mfma_f32_16x16x32_bf16 v[72:75], v[112:115], v[16:19], v[72:75]\n\t"
    "v_mfma_f32_16x16x32_bf16 v[76:79], v[116:119], v[16:19], v[76:79]\n\t"
    "v_mfma_f32_16x16x32_bf16 v[80:83], v[104:107], v[16:19], v[80:83]\n\t"
    "v_mfma_f32_16x16x32_bf16 v[84:87], v[108:111], v[16:19], v[84:87]\n\t"
    "ds_read_b128 v[24:27], v3 offset:17024\n\t"
    "v_mfma_f32_16x16x32_bf16 v[88:91], v[96:99], v[16:19], 0\n\t"
    "v_mfma_f32_16x16x32_bf16 v[92:95], v[100:103], v[16:19], 0\n\t"
    "s_waitcnt lgkmcnt(1)\n\t"
    "v_mfma_f32_16x16x32_bf16 v[40:43], v[160:163], v[20:23], v[40:43]\n\t"
    "v_mfma_f32_16x16x32_bf16 v[44:47], v[164:167], v[20:23], v[44:47]\n\t"
    "v_mfma_f32_16x16x32_bf16 v[48:51], v[152:155], v[20:23], v[48:51]\n\t"
    "v_mfma_f32_16x16x32_bf16 v[52:55], v[156:159], v[20:23], v[52:55]\n\t"
    "ds_read_b128 v[16:19], v3 offset:22400\n\t"
    "v_mfma_f32_16x16x32_bf16 v[56:59], v[144:147], v[20:23], v[56:59]\n\t"
    "v_mfma_f32_16x16x32_bf16 v[60:63], v[148:151], v[20:23], v[60:63]\n\t"
    "s_waitcnt lgkmcnt(1)\n\t"
    "v_mfma_f32_16x16x32_bf16 v[48:51], v[160:163], v[24:27], v[48:51]\n\t"
    "v_mfma_f32_16x16x32_bf16 v[52:55], v[164:167], v[24:27], v[52:55]\n\t"
    "v_mfma_f32_16x16x32_bf16 v[56:59], v[152:155], v[24:27], v[56:59]\n\t"
    "v_mfma_f32_16x16x32_bf16 v[60:63], v[156:159], v[24:27], v[60:63]\n\t"
    "ds_read_b128 v[20:23], v3 offset:27776\n\t"
    "v_mfma_f32_16x16x32_bf16 v[64:67], v[144:147], v[24:27], v[64:67]\n\t"
    "v_mfma_f32_16x16x32_bf16 v[68:71], v[148:151], v[24:27], v[68:71]\n\t"
    "s_setprio 1\n\t"
    "s_waitcnt lgkmcnt(1)\n\t"
    "v_mfma_f32_16x16x32_bf16 v[56:59], v[160:163], v[16:19], v[56:59]\n\t"
    "v_mfma_f32_16x16x32_bf16 v[60:63], v[164:167], v[16:19], v[60:63]\n\t"
    "v_mfma_f32_16x16x32_bf16 v[64:67], v[152:155], v[16:19], v[64:67]\n\t"
    "v_mfma_f32_16x16x32_bf16 v[68:71], v[156:159], v[16:19], v[68:71]\n\t"
    "ds_read_b128 v[24:27], v3 offset:32704\n\t"
    "v_mfma_f32_16x16x32_bf16 v[72:75], v[144:147], v[16:19], v[72:75]\n\t"
    "v_mfma_f32_16x16x32_bf16 v[76:79], v[148:151], v[16:19], v[76:79]\n\t"
    "s_waitcnt lgkmcnt(1)\n\t"
    "v_mfma_f32_16x16x32_bf16 v[64:67], v[160:163], v[20:23], v[64:67]\n\t"
    "v_mfma_f32_16x16x32_bf16 v[68:71], v[164:167], v[20:23], v[68:71]\n\t"
    "v_mfma_f32_16x16x32_bf16 v[72:75], v[152:155], v[20:23], v[72:75]\n\t"
    "v_mfma_f32_16x16x32_bf16 v[76:79], v[156:159], v[20:23], v[76:79]\n\t"
    "ds_read_b128 v[16:19], v3 offset:33152\n\t"
    "v_mfma_f32_16x16x32_bf16 v[80:83], v[144:147], v[20:23], v[80:83]\n\t"
    "v_mfma_f32_16x16x32_bf16 v[84:87], v[148:151], v[20:23], v[84:87]\n\t"
    "s_waitcnt lgkmcnt(1)\n\t"
    "v_mfma_f32_16x16x32_bf16 v[72:75], v[136:139], v[24:27], v[72:75]\n\t"
    "v_mfma_f32_16x16x32_bf16 v[76:79], v[140:143], v[24:27], v[76:79]\n\t"
    "v_mfma_f32_16x16x32_bf16 v[80:83], v[128:131], v[24:27], v[80:83]\n\t"
    "v_mfma_f32_16x16x32_bf16 v[84:87], v[132:135], v[24:27], v[84:87]\n\t"
    "ds_read_b128 v[20:23], v3 offset:37632\n\t"
    "v_mfma_f32_16x16x32_bf16 v[88:91], v[120:123], v[24:27], v[88:91]\n\t"
    "v_mfma_f32_16x16x32_bf16 v[92:95], v[124:127], v[24:27], v[92:95]\n\t"
    "s_waitcnt lgkmcnt(1)\n\t"
    "v_mfma_f32_16x16x32_bf16 v[72:75], v[160:163], v[16:19], v[72:75]\n\t"
    "v_mfma_f32_16x16x32_bf16 v[76:79], v[164:167], v[16:19], v[76:79]\n\t"
    "ds_read_b128 v[24:27], v3 offset:38080\n\t"
    "v_mfma_f32_16x16x32_bf16 v[80:83], v[152:155], v[16:19], v[80:83]\n\t"
    "v_mfma_f32_16x16x32_bf16 v[84:87], v[156:159], v[16:19], v[84:87]\n\t"
    "v_mfma_f32_16x16x32_bf16 v[88:91], v[144:147], v[16:19], v[88:91]\n\t"
    "v_mfma_f32_16x16x32_bf16 v[92:95], v[148:151], v[16:19], v[92:95]\n\t"
    "ds_read_b128 v[124:127], v3 offset:38528\n\t"
    "s_waitcnt lgkmcnt(2)\n\t"
    "v_mfma_f32_16x16x32_bf16 v[80:83], v[112:115], v[20:23], v[80:83]\n\t"
    "v_mfma_f32_16x16x32_bf16 v[84:87], v[116:119], v[20:23], v[84:87]\n\t"
    "v_mfma_f32_16x16x32_bf16 v[88:91], v[104:107], v[20:23], v[88:91]\n\t"
    "v_mfma_f32_16x16x32_bf16 v[92:95], v[108:111], v[20:23], v[92:95]\n\t"
    "ds_read_b128 v[16:19], v3 offset:43008\n\t"
    "s_waitcnt lgkmcnt(2)\n\t"
    "v_mfma_f32_16x16x32_bf16 v[80:83], v[136:139], v[24:27], v[80:83]\n\t"
    "v_mfma_f32_16x16x32_bf16 v[84:87], v[140:143], v[24:27], v[84:87]\n\t"
    "ds_read_b128 v[20:23], v3 offset:43456\n\t"
    "v_mfma_f32_16x16x32_bf16 v[88:91], v[128:131], v[24:27], v[88:91]\n\t"
    "v_mfma_f32_16x16x32_bf16 v[92:95], v[132:135], v[24:27], v[92:95]\n\t"
    "ds_read_b128 v[108:111], v3 offset:43904\n\t"
    "s_waitcnt lgkmcnt(3)\n\t"
    "v_mfma_f32_16x16x32_bf16 v[80:83], v[160:163], v[124:127], v[80:83]\n\t"
    "v_mfma_f32_16x16x32_bf16 v[84:87], v[164:167], v[124:127], v[84:87]\n\t"
    "v_mfma_f32_16x16x32_bf16 v[88:91], v[152:155], v[124:127], v[88:91]\n\t"
    "v_mfma_f32_16x16x32_bf16 v[92:95], v[156:159], v[124:127], v[92:95]\n\t"
    "s_waitcnt lgkmcnt(2)\n\t"
    "v_mfma_f32_16x16x32_bf16 v[88:91], v[112:115], v[16:19], v[88:91]\n\t"
    "ds_read_b32 v8, v7\n\t"
    "v_mfma_f32_16x16x32_bf16 v[92:95], v[116:119], v[16:19], v[92:95]\n\t"
    "s_waitcnt lgkmcnt(2)\n\t"
    "v_mfma_f32_16x16x32_bf16 v[88:91], v[136:139], v[20:23], v[88:91]\n\t"
    "v_mfma_f32_16x16x32_bf16 v[92:95], v[140:143], v[20:23], v[92:95]\n\t"
    "s_waitcnt lgkmcnt(1)\n\t"
    "v_mfma_f32_16x16x32_bf16 v[88:91], v[160:163], v[108:111], v[88:91]\n\t"
    "v_mfma_f32_16x16x32_bf16 v[92:95], v[164:167], v[108:111], v[92:95]\n\t"
    "s_waitcnt lgkmcnt(0)\n\t"
    "v_cmp_lt_u32 vcc, 2, v8\n\t"
    "s_and_b32 s44, vcc_lo, s38\n\t"
    "s_cbranch_scc1 L_slow72\n\t"
    "L_cont72:\n\t"
    "ds_read_b128 v[164:167], v11 offset:18816\n\t"
    "ds_read_b128 v[160:163], v11 offset:19264\n\t"
    "v_cmp_lt_u32 vcc, 1, v8\n\t"
    "s_and_b32 s44, vcc_lo, s38\n\t"
    "s_cbranch_scc1 L_slow73\n\t"
    "L_cont73:\n\t"
    "ds_read_b128 v[140:143], v11 offset:21504\n\t"
    "ds_read_b128 v[136:139], v11 offset:21952\n\t"
    "s_waitcnt lgkmcnt(3)\n\t"
    "v_mfma_f32_16x16x32_bf16 v[40:43], v[12:15], v[164:167], v[40:43]\n\t"
    "s_waitcnt lgkmcnt(2)\n\t"
    "v_mfma_f32_16x16x32_bf16 v[44:47], v[12:15], v[160:163], v[44:47]\n\t"
    "v_cmp_lt_u32 vcc, 0, v8\n\t"
    "s_and_b32 s44, vcc_lo, s38\n\t"
    "s_cbranch_scc1 L_slow74\n\t"
    "L_cont74:\n\t"
    "ds_read_b128 v[160:163], v11 offset:24192\n\t"
    "ds_read_b128 v[164:167], v11 offset:24640\n\t"
    "s_waitcnt lgkmcnt(3)\n\t"
    "v_mfma_f32_16x16x32_bf16 v[48:51], v[12:15], v[140:143], v[48:51]\n\t"
    "s_waitcnt lgkmcnt(2)\n\t"
    "v_mfma_f32_16x16x32_bf16 v[52:55], v[12:15], v[136:139], v[52:55]\n\t"
    "v_cvt_pk_bf16_f32 v40, v40, v41\n\t"
    "v_cvt_pk_bf16_f32 v41, v42, v43\n\t"
    "v_cvt_pk_bf16_f32 v42, v44, v45\n\t"
    "v_cvt_pk_bf16_f32 v43, v46, v47\n\t"
    "s_nop 0\n\t"
    "v_permlane16_swap_b32 v40, v42\n\t"
    "v_permlane16_swap_b32 v41, v43\n\t"
    "buffer_store_dwordx4 v[40:43], v4, s[24:27], s39 offen sc0 sc1\n\t"
    "s_add_u32 s39, s39, 43008\n\t"
    "s_nop 0\n\t"
    "ds_read_b128 v[40:43], v11 offset:26880\n\t"
    "ds_read_b128 v[44:47], v11 offset:27328\n\t"
    "s_waitcnt lgkmcnt(3)\n\t"
    "v_mfma_f32_16x16x32_bf16 v[56:59], v[12:15], v[160:163], v[56:59]\n\t"
    "s_waitcnt lgkmcnt(2)\n\t"
    "v_mfma_f32_16x16x32_bf16 v[60:63], v[12:15], v[164:167], v[60:63]\n\t"
    "v_cvt_pk_bf16_f32 v48, v48, v49\n\t"
    "v_cvt_pk_bf16_f32 v49, v50, v51\n\t"
    "v_cvt_pk_bf16_f32 v50, v52, v53\n\t"
    "v_cvt_pk_bf16_f32 v51, v54, v55\n\t"
    "s_nop 0\n\t"
    "v_permlane16_swap_b32 v48, v50\n\t"
    "v_permlane16_swap_b32 v49, v51\n\t"
    "buffer_store_dwordx4 v[48:51], v4, s[24:27], s39 offen sc0 sc1\n\t"
    "s_add_u32 s39, s39, 43008\n\t"
    "s_nop 0\n\t"
    "ds_read_b128 v[48:51], v11 offset:29568\n\t"
    "ds_read_b128 v[52:55], v11 offset:30016\n\t"
    "s_waitcnt lgkmcnt(3)\n\t"
    "v_mfma_f32_16x16x32_bf16 v[64:67], v[12:15], v[40:43], v[64:67]\n\t"
    "s_waitcnt lgkmcnt(2)\n\t"
    "v_mfma_f32_16x16x32_bf16 v[68:71], v[12:15], v[44:47], v[68:71]\n\t"
    "v_cvt_pk_bf16_f32 v56, v56, v57\n\t"
    "v_cvt_pk_bf16_f32 v57, v58, v59\n\t"
    "v_cvt_pk_bf16_f32 v58, v60, v61\n\t"
    "v_cvt_pk_bf16_f32 v59, v62, v63\n\t"
    "s_nop 0\n\t"
    "v_permlane16_swap_b32 v56, v58\n\t"
    "v_permlane16_swap_b32 v57, v59\n\t"
    "buffer_store_dwordx4 v[56:59], v4, s[24:27], s39 offen sc0 sc1\n\t"
    "s_add_u32 s39, s39, 43008\n\t"
    "s_nop 0\n\t"
    "ds_read_b128 v[56:59], v11 offset:32256\n\t"
    "ds_read_b128 v[60:63], v11 offset:32704\n\t"
    "s_waitcnt lgkmcnt(3)\n\t"
    "v_mfma_f32_16x16x32_bf16 v[72:75], v[12:15], v[48:51], v[72:75]\n\t"
    "s_waitcnt lgkmcnt(2)\n\t"
    "v_mfma_f32_16x16x32_bf16 v[76:79], v[12:15], v[52:55], v[76:79]\n\t"
    "v_cvt_pk_bf16_f32 v64, v64, v65\n\t"
    "v_cvt_pk_bf16_f32 v65, v66, v67\n\t"
    "v_cvt_pk_bf16_f32 v66, v68, v69\n\t"
    "v_cvt_pk_bf16_f32 v67, v70, v71\n\t"
    "s_nop 0\n\t"
    "v_permlane16_swap_b32 v64, v66\n\t"
    "v_permlane16_swap_b32 v65, v67\n\t"
    "buffer_store_dwordx4 v[64:67], v4, s[24:27], s39 offen sc0 sc1\n\t"
    "s_add_u32 s39, s39, 43008\n\t"
    "s_nop 0\n\t"
    "ds_read_b128 v[64:67], v11 offset:34944\n\t"
    "ds_read_b128 v[68:71], v11 offset:35392\n\t"
    "s_waitcnt lgkmcnt(3)\n\t"
    "v_mfma_f32_16x16x32_bf16 v[80:83], v[12:15], v[56:59], v[80:83]\n\t"
    "s_waitcnt lgkmcnt(2)\n\t"
    "v_mfma_f32_16x16x32_bf16 v[84:87], v[12:15], v[60:63], v[84:87]\n\t"
    "v_cvt_pk_bf16_f32 v72, v72, v73\n\t"
    "v_cvt_pk_bf16_f32 v73, v74, v75\n\t"
    "v_cvt_pk_bf16_f32 v74, v76, v77\n\t"
    "v_cvt_pk_bf16_f32 v75, v78, v79\n\t"
    "s_nop 0\n\t"
    "v_permlane16_swap_b32 v72, v74\n\t"
    "v_permlane16_swap_b32 v73, v75\n\t"
    "buffer_store_dwordx4 v[72:75], v4, s[24:27], s39 offen sc0 sc1\n\t"
    "s_add_u32 s39, s39, 43008\n\t"
    "s_waitcnt lgkmcnt(1)\n\t"
    "v_mfma_f32_16x16x32_bf16 v[88:91], v[12:15], v[64:67], v[88:91]\n\t"
    "s_waitcnt lgkmcnt(0)\n\t"
    "v_mfma_f32_16x16x32_bf16 v[92:95], v[12:15], v[68:71], v[92:95]\n\t"
    "v_cvt_pk_bf16_f32 v80, v80, v81\n\t"
    "v_cvt_pk_bf16_f32 v81, v82, v83\n\t"
    "v_cvt_pk_bf16_f32 v82, v84, v85\n\t"
    "v_cvt_pk_bf16_f32 v83, v86, v87\n\t"
    "s_nop 0\n\t"
    "v_permlane16_swap_b32 v80, v82\n\t"
    "v_permlane16_swap_b32 v81, v83\n\t"
    "buffer_store_dwordx4 v[80:83], v4, s[24:27], s39 offen sc0 sc1\n\t"
    "s_add_u32 s39, s39, 43008\n\t"
    "v_cvt_pk_bf16_f32 v88, v88, v89\n\t"
    "v_cvt_pk_bf16_f32 v89, v90, v91\n\t"
    "v_cvt_pk_bf16_f32 v90, v92, v93\n\t"
    "v_cvt_pk_bf16_f32 v91, v94, v95\n\t"
    "s_nop 0\n\t"
    "v_permlane16_swap_b32 v88, v90\n\t"
    "v_permlane16_swap_b32 v89, v91\n\t"
    "buffer_store_dwordx4 v[88:91], v4, s[24:27], s39 offen sc0 sc1\n\t"
    "L_finE:\n\t"
    "s_branch L_end\n\t"
    "L_poll10:\n\t"
    "s_sleep 1\n\t"
    "ds_read_b32 v8, v7\n\t"
    "s_sub_u32 s43, s43, 1\n\t"
    "s_cbranch_scc1 L_end\n\t"
    "s_waitcnt lgkmcnt(0)\n\t"
    "v_cmp_lt_u32 vcc, 0, v8\n\t"
    "s_and_b32 s44, vcc_lo, 0xf00000\n\t"
    "s_cbranch_scc1 L_poll10\n\t"
    "ds_read_b64_tr_b16 v[96:97], v5 offset:0\n\t"
    "ds_read_b64_tr_b16 v[98:99], v5 offset:768\n\t"
    "ds_read_b64_tr_b16 v[100:101], v6 offset:0\n\t"
    "ds_read_b64_tr_b16 v[102:103], v6 offset:768\n\t"
    "s_branch L_polld10\n\t"
    "L_slow12:\n\t"
    "ds_read_b32 v8, v7\n\t"
    "s_waitcnt lgkmcnt(0)\n\t"
    "v_cmp_lt_u32 vcc, 8, v8\n\t"
    "s_and_b32 s44, vcc_lo, 0xfff\n\t"
    "s_cbranch_scc0 L_cont12\n\t"
    "s_sleep 1\n\t"
    "s_sub_u32 s43, s43, 1\n\t"
    "s_cbranch_scc1 L_end\n\t"
    "s_branch L_slow12\n\t"
    "L_slow13:\n\t"
    "ds_read_b32 v8, v7\n\t"
    "s_waitcnt lgkmcnt(0)\n\t"
    "v_cmp_lt_u32 vcc, 0, v8\n\t"
    "s_and_b32 s44, vcc_lo, 0xf000000\n\t"
    "s_cbranch_scc0 L_cont13\n\t"
    "s_sleep 1\n\t"
    "s_sub_u32 s43, s43, 1\n\t"
    "s_cbranch_scc1 L_end\n\t"
    "s_branch L_slow13\n\t"
    "L_slow14:\n\t"
    "ds_read_b32 v8, v7\n\t"
    "s_waitcnt lgkmcnt(0)\n\t"
    "v_cmp_lt_u32 vcc, 7, v8\n\t"
    "s_and_b32 s44, vcc_lo, 0xfff\n\t"
    "s_cbranch_scc0 L_cont14\n\t"
    "s_sleep 1\n\t"
    "s_sub_u32 s43, s43, 1\n\t"
    "s_cbranch_scc1 L_end\n\t"
    "s_branch L_slow14\n\t"
    "L_slow15:\n\t"
    "ds_read_b32 v8, v7\n\t"
    "s_waitcnt lgkmcnt(0)\n\t"
    "v_cmp_lt_u32 vcc, 0, v8\n\t"
    "s_and_b32 s44, vcc_lo, 0xf0000000\n\t"
    "s_cbranch_scc0 L_cont15\n\t"
    "s_sleep 1\n\t"
    "s_sub_u32 s43, s43, 1\n\t"
    "s_cbranch_scc1 L_end\n\t"
    "s_branch L_slow15\n\t"
    "L_slow16:\n\t"
    "ds_read_b32 v8, v7\n\t"
    "s_waitcnt lgkmcnt(0)\n\t"
    "v_cmp_lt_u32 vcc, 6, v8\n\t"
    "s_and_b32 s44, vcc_lo, 0xfff\n\t"
    "s_cbranch_scc0 L_cont16\n\t"
    "s_sleep 1\n\t"
    "s_sub_u32 s43, s43, 1\n\t"
    "s_cbranch_scc1 L_end\n\t"
    "s_branch L_slow16\n\t"
    "L_slow17:\n\t"
    "ds_read_b32 v8, v7\n\t"
    "s_waitcnt lgkmcnt(0)\n\t"
    "v_cmp_lt_u32 vcc, 0, v8\n\t"
    "s_and_b32 s44, vcc_hi, 0xf\n\t"
    "s_cbranch_scc0 L_cont17\n\t"
    "s_sleep 1\n\t"
    "s_sub_u32 s43, s43, 1\n\t"
    "s_cbranch_scc1 L_end\n\t"
    "s_branch L_slow17\n\t"
    "L_slow18:\n\t"
    "ds_read_b32 v8, v7\n\t"
    "s_waitcnt lgkmcnt(0)\n\t"
    "v_cmp_lt_u32 vcc, 5, v8\n\t"
    "s_and_b32 s44, vcc_lo, 0xfff\n\t"
    "s_cbranch_scc0 L_cont18\n\t"
    "s_sleep 1\n\t"
    "s_sub_u32 s43, s43, 1\n\t"
    "s_cbranch_scc1 L_end\n\t"
    "s_branch L_slow18\n\t"
    "L_slow19:\n\t"
    "ds_read_b32 v8, v7\n\t"
    "s_waitcnt lgkmcnt(0)\n\t"
    "v_cmp_lt_u32 vcc, 0, v8\n\t"
    "s_and_b32 s44, vcc_hi, 0xf0\n\t"
    "s_cbranch_scc0 L_cont19\n\t"
    "s_sleep 1\n\t"
    "s_sub_u32 s43, s43, 1\n\t"
    "s_cbranch_scc1 L_end\n\t"
    "s_branch L_slow19\n\t"
    "L_slow20:\n\t"
    "ds_read_b32 v8, v7\n\t"
    "s_waitcnt lgkmcnt(0)\n\t"
    "v_cmp_lt_u32 vcc, 4, v8\n\t"
    "s_and_b32 s44, vcc_lo, 0xfff\n\t"
    "s_cbranch_scc0 L_cont20\n\t"
    "s_sleep 1\n\t"
    "s_sub_u32 s43, s43, 1\n\t"
    "s_cbranch_scc1 L_end\n\t"
    "s_branch L_slow20\n\t"
    "L_slow21:\n\t"
    "ds_read_b32 v8, v7\n\t"
    "s_waitcnt lgkmcnt(0)\n\t"
    "v_cmp_lt_u32 vcc, 0, v8\n\t"
    "s_and_b32 s44, vcc_hi, 0xf00\n\t"
    "s_cbranch_scc0 L_cont21\n\t"
    "s_sleep 1\n\t"
    "s_sub_u32 s43, s43, 1\n\t"
    "s_cbranch_scc1 L_end\n\t"
    "s_branch L_slow21\n\t"
    "L_poll22:\n\t"
    "s_sleep 1\n\t"
    "ds_read_b32 v8, v7\n\t"
    "s_sub_u32 s43, s43, 1\n\t"
    "s_cbranch_scc1 L_end\n\t"
    "s_waitcnt lgkmcnt(0)\n\t"
    "v_cmp_lt_u32 vcc, 0, v8\n\t"
    "s_and_b32 s44, vcc_hi, 0xf000\n\t"
    "s_cbranch_scc1 L_poll22\n\t"
    "ds_read_b64_tr_b16 v[144:145], v5 offset:37632\n\t"
    "ds_read_b64_tr_b16 v[146:147], v5 offset:38400\n\t"
    "ds_read_b64_tr_b16 v[148:149], v6 offset:37632\n\t"
    "ds_read_b64_tr_b16 v[150:151], v6 offset:38400\n\t"
    "s_branch L_polld22\n\t"
    "L_poll23:\n\t"
    "s_sleep 1\n\t"
    "ds_read_b32 v8, v7\n\t"
    "s_sub_u32 s43, s43, 1\n\t"
    "s_cbranch_scc1 L_end\n\t"
    "s_waitcnt lgkmcnt(0)\n\t"
    "v_cmp_lt_u32 vcc, 0, v8\n\t"
    "s_and_b32 s44, vcc_hi, 0xf0000\n\t"
    "s_cbranch_scc1 L_poll23\n\t"
    "ds_read_b64_tr_b16 v[152:153], v5 offset:56448\n\t"
    "ds_read_b64_tr_b16 v[154:155], v5 offset:57216\n\t"
    "ds_read_b64_tr_b16 v[156:157], v6 offset:56448\n\t"
    "ds_read_b64_tr_b16 v[158:159], v6 offset:57216\n\t"
    "s_branch L_polld23\n\t"
    "L_poll24:\n\t"
    "s_sleep 1\n\t"
    "ds_read_b32 v8, v7\n\t"
    "s_sub_u32 s43, s43, 1\n\t"
    "s_cbranch_scc1 L_end\n\t"
    "s_waitcnt lgkmcnt(0)\n\t"
    "v_cmp_lt_u32 vcc, 0, v8\n\t"
    "s_and_b32 s44, vcc_hi, 0xf00000\n\t"
    "s_cbranch_scc1 L_poll24\n\t"
    "ds_read_b64_tr_b16 v[160:161], v5 offset:0\n\t"
    "ds_read_b64_tr_b16 v[162:163], v5 offset:768\n\t"
    "ds_read_b64_tr_b16 v[164:165], v6 offset:0\n\t"
    "ds_read_b64_tr_b16 v[166:167], v6 offset:768\n\t"
    "s_branch L_polld24\n\t"
    "L_slow25:\n\t"
    "ds_read_b32 v8, v7\n\t"
    "s_waitcnt lgkmcnt(0)\n\t"
    "v_cmp_lt_u32 vcc, 0, v8\n\t"
    "s_and_b32 s44, vcc_hi, 0xff000000\n\t"
    "s_cbranch_scc0 L_cont25\n\t"
    "s_sleep 1\n\t"
    "s_sub_u32 s43, s43, 1\n\t"
    "s_cbranch_scc1 L_end\n\t"
    "s_branch L_slow25\n\t"
    "L_slow26:\n\t"
    "ds_read_b32 v8, v7\n\t"
    "s_waitcnt lgkmcnt(0)\n\t"
    "v_cmp_lt_u32 vcc, 2, v8\n\t"
    "s_and_b32 s44, vcc_lo, s38\n\t"
    "s_cbranch_scc0 L_cont26\n\t"
    "s_sleep 1\n\t"
    "s_sub_u32 s43, s43, 1\n\t"
    "s_cbranch_scc1 L_end\n\t"
    "s_branch L_slow26\n\t"
    "L_slow27:\n\t"
    "ds_read_b32 v8, v7\n\t"
    "s_waitcnt lgkmcnt(0)\n\t"
    "v_cmp_lt_u32 vcc, 1, v8\n\t"
    "s_and_b32 s44, vcc_lo, s38\n\t"
    "s_cbranch_scc0 L_cont27\n\t"
    "s_sleep 1\n\t"
    "s_sub_u32 s43, s43, 1\n\t"
    "s_cbranch_scc1 L_end\n\t"
    "s_branch L_slow27\n\t"
    "L_slow28:\n\t"
    "ds_read_b32 v8, v7\n\t"
    "s_waitcnt lgkmcnt(0)\n\t"
    "v_cmp_lt_u32 vcc, 0, v8\n\t"
    "s_and_b32 s44, vcc_lo, s38\n\t"
    "s_cbranch_scc0 L_cont28\n\t"
    "s_sleep 1\n\t"
    "s_sub_u32 s43, s43, 1\n\t"
    "s_cbranch_scc1 L_end\n\t"
    "s_branch L_slow28\n\t"
    "L_poll30:\n\t"
    "s_sleep 1\n\t"
    "ds_read_b32 v8, v7\n\t"
    "s_sub_u32 s43, s43, 1\n\t"
    "s_cbranch_scc1 L_end\n\t"
    "s_waitcnt lgkmcnt(0)\n\t"
    "v_cmp_lt_u32 vcc, 0, v8\n\t"
    "s_and_b32 s44, vcc_lo, 0xf0000\n\t"
    "s_cbranch_scc1 L_poll30\n\t"
    "ds_read_b128 v[156:159], v124 offset:0\n\t"
    "ds_read_b128 v[164:167], v125 offset:0\n\t"
    "ds_read_b128 v[88:91], v124 offset:64\n\t"
    "ds_read_b128 v[92:95], v125 offset:64\n\t"
    "ds_read_b128 v[112:115], v124 offset:128\n\t"
    "ds_read_b128 v[116:119], v125 offset:128\n\t"
    "s_branch L_polld30\n\t"
    "L_poll31:\n\t"
    "s_sleep 1\n\t"
    "ds_read_b32 v8, v7 offset:256\n\t"
    "s_sub_u32 s43, s43, 1\n\t"
    "s_cbranch_scc1 L_end\n\t"
    "s_waitcnt lgkmcnt(0)\n\t"
    "v_cmp_lt_u32 vcc, 0, v8\n\t"
    "s_and_b32 s44, vcc_lo, s48\n\t"
    "s_cbranch_scc1 L_poll31\n\t"
    "ds_read_b128 v[128:131], v120 offset:192\n\t"
    "ds_read_b128 v[140:143], v121 offset:0\n\t"
    "ds_read_b128 v[132:135], v120 offset:256\n\t"
    "ds_read_b128 v[144:147], v121 offset:64\n\t"
    "ds_read_b128 v[136:139], v120 offset:320\n\t"
    "ds_read_b128 v[148:151], v121 offset:128\n\t"
    "s_branch L_polld31\n\t"
    "L_slow32:\n\t"
    "ds_read_b32 v8, v7\n\t"
    "s_waitcnt lgkmcnt(0)\n\t"
    "v_cmp_lt_u32 vcc, 0, v8\n\t"
    "s_and_b32 s44, vcc_lo, 0xf00000\n\t"
    "s_cbranch_scc0 L_cont32\n\t"
    "s_sleep 1\n\t"
    "s_sub_u32 s43, s43, 1\n\t"
    "s_cbranch_scc1 L_end\n\t"
    "s_branch L_slow32\n\t"
    "L_slow33:\n\t"
    "ds_read_b32 v8, v7\n\t"
    "s_waitcnt lgkmcnt(0)\n\t"
    "v_cmp_lt_u32 vcc, 0, v8\n\t"
    "s_and_b32 s44, vcc_lo, 0xf000000\n\t"
    "s_cbranch_scc0 L_cont33\n\t"
    "s_sleep 1\n\t"
    "s_sub_u32 s43, s43, 1\n\t"
    "s_cbranch_scc1 L_end\n\t"
    "s_branch L_slow33\n\t"
    "L_slow34:\n\t"
    "ds_read_b32 v8, v7\n\t"
    "s_waitcnt lgkmcnt(0)\n\t"
    "v_cmp_lt_u32 vcc, 0, v8\n\t"
    "s_and_b32 s44, vcc_lo, 0xf0000000\n\t"
    "s_cbranch_scc0 L_cont34\n\t"
    "s_sleep 1\n\t"
    "s_sub_u32 s43, s43, 1\n\t"
    "s_cbranch_scc1 L_end\n\t"
    "s_branch L_slow34\n\t"
    "L_slow35:\n\t"
    "ds_read_b32 v8, v7\n\t"
    "s_waitcnt lgkmcnt(0)\n\t"
    "v_cmp_lt_u32 vcc, 3, v8\n\t"
    "s_and_b32 s44, vcc_hi, 0x3f000000\n\t"
    "s_cbranch_scc0 L_cont35\n\t"
    "s_sleep 1\n\t"
    "s_sub_u32 s43, s43, 1\n\t"
    "s_cbranch_scc1 L_end\n\t"
    "s_branch L_slow35\n\t"
    "L_slow36:\n\t"
    "ds_read_b32 v8, v7\n\t"
    "s_waitcnt lgkmcnt(0)\n\t"
    "v_cmp_lt_u32 vcc, 0, v8\n\t"
    "s_and_b32 s44, vcc_hi, 0xf\n\t"
    "s_cbranch_scc0 L_cont36\n\t"
    "s_sleep 1\n\t"
    "s_sub_u32 s43, s43, 1\n\t"
    "s_cbranch_scc1 L_end\n\t"
    "s_branch L_slow36\n\t"
    "L_slow37:\n\t"
    "ds_read_b32 v8, v7\n\t"
    "s_waitcnt lgkmcnt(0)\n\t"
    "v_cmp_lt_u32 vcc, 1, v8\n\t"
    "s_and_b32 s44, vcc_hi, 0xff000000\n\t"
    "s_cbranch_scc0 L_cont37\n\t"
    "s_sleep 1\n\t"
    "s_sub_u32 s43, s43, 1\n\t"
    "s_cbranch_scc1 L_end\n\t"
    "s_branch L_slow37\n\t"
    "L_slow38:\n\t"
    "ds_read_b32 v8, v7\n\t"
    "s_waitcnt lgkmcnt(0)\n\t"
    "v_cmp_lt_u32 vcc, 0, v8\n\t"
    "s_and_b32 s44, vcc_hi, 0xf0\n\t"
    "s_cbranch_scc0 L_cont38\n\t"
    "s_sleep 1\n\t"
    "s_sub_u32 s43, s43, 1\n\t"
    "s_cbranch_scc1 L_end\n\t"
    "s_branch L_slow38\n\t"
    "L_slow39:\n\t"
    "ds_read_b32 v8, v7\n\t"
    "s_waitcnt lgkmcnt(0)\n\t"
    "v_cmp_lt_u32 vcc, 0, v8\n\t"
    "s_and_b32 s44, vcc_hi, 0xf00\n\t"
    "s_cbranch_scc0 L_cont39\n\t"
    "s_sleep 1\n\t"
    "s_sub_u32 s43, s43, 1\n\t"
    "s_cbranch_scc1 L_end\n\t"
    "s_branch L_slow39\n\t"
    "L_slow40:\n\t"
    "ds_read_b32 v8, v7\n\t"
    "s_waitcnt lgkmcnt(0)\n\t"
    "v_cmp_lt_u32 vcc, 0, v8\n\t"
    "s_and_b32 s44, vcc_hi, 0xf000\n\t"
    "s_cbranch_scc0 L_cont40\n\t"
    "s_sleep 1\n\t"
    "s_sub_u32 s43, s43, 1\n\t"
    "s_cbranch_scc1 L_end\n\t"
    "s_branch L_slow40\n\t"
    "L_slow41:\n\t"
    "ds_read_b32 v8, v7\n\t"
    "s_waitcnt lgkmcnt(0)\n\t"
    "v_cmp_lt_u32 vcc, 0, v8\n\t"
    "s_and_b32 s44, vcc_hi, 0xf0000\n\t"
    "s_cbranch_scc0 L_cont41\n\t"
    "s_sleep 1\n\t"
    "s_sub_u32 s43, s43, 1\n\t"
    "s_cbranch_scc1 L_end\n\t"
    "s_branch L_slow41\n\t"
    "L_slow42:\n\t"
    "ds_read_b32 v8, v7\n\t"
    "s_waitcnt lgkmcnt(0)\n\t"
    "v_cmp_lt_u32 vcc, 0, v8\n\t"
    "s_and_b32 s44, vcc_hi, 0xf00000\n\t"
    "s_cbranch_scc0 L_cont42\n\t"
    "s_sleep 1\n\t"
    "s_sub_u32 s43, s43, 1\n\t"
    "s_cbranch_scc1 L_end\n\t"
    "s_branch L_slow42\n\t"
    "L_slow43:\n\t"
    "ds_read_b32 v8, v7\n\t"
    "s_waitcnt lgkmcnt(0)\n\t"
    "v_cmp_lt_u32 vcc, 3, v8\n\t"
    "s_and_b32 s44, vcc_lo, 0xfff\n\t"
    "s_cbranch_scc0 L_cont43\n\t"
    "s_sleep 1\n\t"
    "s_sub_u32 s43, s43, 1\n\t"
    "s_cbranch_scc1 L_end\n\t"
    "s_branch L_slow43\n\t"
    "L_poll45:\n\t"
    "s_sleep 1\n\t"
    "ds_read_b32 v8, v7\n\t"
    "s_sub_u32 s43, s43, 1\n\t"
    "s_cbranch_scc1 L_end\n\t"
    "s_waitcnt lgkmcnt(0)\n\t"
    "v_cmp_lt_u32 vcc, 0, v8\n\t"
    "s_and_b32 s44, vcc_lo, 0xf0000\n\t"
    "s_cbranch_scc1 L_poll45\n\t"
    "ds_read_b128 v[156:159], v124 offset:0\n\t"
    "ds_read_b128 v[164:167], v125 offset:0\n\t"
    "ds_read_b128 v[88:91], v124 offset:64\n\t"
    "ds_read_b128 v[92:95], v125 offset:64\n\t"
    "ds_read_b128 v[112:115], v124 offset:128\n\t"
    "ds_read_b128 v[116:119], v125 offset:128\n\t"
    "s_branch L_polld45\n\t"
    "L_poll46:\n\t"
    "s_sleep 1\n\t"
    "ds_read_b32 v8, v7\n\t"
    "s_sub_u32 s43, s43, 1\n\t"
    "s_cbranch_scc1 L_end\n\t"
    "s_waitcnt lgkmcnt(0)\n\t"
    "v_cmp_lt_u32 vcc, 0, v8\n\t"
    "s_and_b32 s44, vcc_lo, 0xf00000\n\t"
    "s_cbranch_scc1 L_poll46\n\t"
    "ds_read_b64_tr_b16 v[128:129], v5 offset:0\n\t"
    "ds_read_b64_tr_b16 v[130:131], v5 offset:3072\n\t"
    "ds_read_b64_tr_b16 v[132:133], v5 offset:384\n\t"
    "ds_read_b64_tr_b16 v[134:135], v5 offset:3456\n\t"
    "ds_read_b64_tr_b16 v[136:137], v5 offset:768\n\t"
    "ds_read_b64_tr_b16 v[138:139], v5 offset:3840\n\t"
    "ds_read_b64_tr_b16 v[140:141], v5 offset:1152\n\t"
    "ds_read_b64_tr_b16 v[142:143], v5 offset:4224\n\t"
    "s_branch L_polld46\n\t"
    "L_poll47:\n\t"
    "s_sleep 1\n\t"
    "ds_read_b32 v8, v7 offset:256\n\t"
    "s_sub_u32 s43, s43, 1\n\t"
    "s_cbranch_scc1 L_end\n\t"
    "s_waitcnt lgkmcnt(0)\n\t"
    "v_cmp_lt_u32 vcc, 0, v8\n\t"
    "s_and_b32 s44, vcc_lo, s48\n\t"
    "s_cbranch_scc1 L_poll47\n\t"
    "ds_read_b128 v[128:131], v120 offset:192\n\t"
    "ds_read_b128 v[140:143], v121 offset:0\n\t"
    "ds_read_b128 v[132:135], v120 offset:256\n\t"
    "ds_read_b128 v[144:147], v121 offset:64\n\t"
    "ds_read_b128 v[136:139], v120 offset:320\n\t"
    "ds_read_b128 v[148:151], v121 offset:128\n\t"
    "s_branch L_polld47\n\t"
    "L_slow48:\n\t"
    "ds_read_b32 v8, v7\n\t"
    "s_waitcnt lgkmcnt(0)\n\t"
    "v_cmp_lt_u32 vcc, 0, v8\n\t"
    "s_and_b32 s44, vcc_lo, 0xf000000\n\t"
    "s_cbranch_scc0 L_cont48\n\t"
    "s_sleep 1\n\t"
    "s_sub_u32 s43, s43, 1\n\t"
    "s_cbranch_scc1 L_end\n\t"
    "s_branch L_slow48\n\t"
    "L_slow49:\n\t"
    "ds_read_b32 v8, v7\n\t"
    "s_waitcnt lgkmcnt(0)\n\t"
    "v_cmp_lt_u32 vcc, 0, v8\n\t"
    "s_and_b32 s44, vcc_lo, 0xf0000000\n\t"
    "s_cbranch_scc0 L_cont49\n\t"
    "s_sleep 1\n\t"
    "s_sub_u32 s43, s43, 1\n\t"
    "s_cbranch_scc1 L_end\n\t"
    "s_branch L_slow49\n\t"
    "L_slow50:\n\t"
    "ds_read_b32 v8, v7\n\t"
    "s_waitcnt lgkmcnt(0)\n\t"
    "v_cmp_lt_u32 vcc, 3, v8\n\t"
    "s_and_b32 s44, vcc_hi, 0x3f000000\n\t"
    "s_cbranch_scc0 L_cont50\n\t"
    "s_sleep 1\n\t"
    "s_sub_u32 s43, s43, 1\n\t"
    "s_cbranch_scc1 L_end\n\t"
    "s_branch L_slow50\n\t"
    "L_slow51:\n\t"
    "ds_read_b32 v8, v7\n\t"
    "s_waitcnt lgkmcnt(0)\n\t"
    "v_cmp_lt_u32 vcc, 0, v8\n\t"
    "s_and_b32 s44, vcc_hi, 0xf\n\t"
    "s_cbranch_scc0 L_cont51\n\t"
    "s_sleep 1\n\t"
    "s_sub_u32 s43, s43, 1\n\t"
    "s_cbranch_scc1 L_end\n\t"
    "s_branch L_slow51\n\t"
    "L_slow52:\n\t"
    "ds_read_b32 v8, v7\n\t"
    "s_waitcnt lgkmcnt(0)\n\t"
    "v_cmp_lt_u32 vcc, 1, v8\n\t"
    "s_and_b32 s44, vcc_hi, 0xff000000\n\t"
    "s_cbranch_scc0 L_cont52\n\t"
    "s_sleep 1\n\t"
    "s_sub_u32 s43, s43, 1\n\t"
    "s_cbranch_scc1 L_end\n\t"
    "s_branch L_slow52\n\t"
    "L_slow53:\n\t"
    "ds_read_b32 v8, v7\n\t"
    "s_waitcnt lgkmcnt(0)\n\t"
    "v_cmp_lt_u32 vcc, 0, v8\n\t"
    "s_and_b32 s44, vcc_hi, 0xf0\n\t"
    "s_cbranch_scc0 L_cont53\n\t"
    "s_sleep 1\n\t"
    "s_sub_u32 s43, s43, 1\n\t"
    "s_cbranch_scc1 L_end\n\t"
    "s_branch L_slow53\n\t"
    "L_slow54:\n\t"
    "ds_read_b32 v8, v7\n\t"
    "s_waitcnt lgkmcnt(0)\n\t"
    "v_cmp_lt_u32 vcc, 0, v8\n\t"
    "s_and_b32 s44, vcc_hi, 0xf00\n\t"
    "s_cbranch_scc0 L_cont54\n\t"
    "s_sleep 1\n\t"
    "s_sub_u32 s43, s43, 1\n\t"
    "s_cbranch_scc1 L_end\n\t"
    "s_branch L_slow54\n\t"
    "L_slow55:\n\t"
    "ds_read_b32 v8, v7\n\t"
    "s_waitcnt lgkmcnt(0)\n\t"
    "v_cmp_lt_u32 vcc, 0, v8\n\t"
    "s_and_b32 s44, vcc_hi, 0xf000\n\t"
    "s_cbranch_scc0 L_cont55\n\t"
    "s_sleep 1\n\t"
    "s_sub_u32 s43, s43, 1\n\t"
    "s_cbranch_scc1 L_end\n\t"
    "s_branch L_slow55\n\t"
    "L_slow56:\n\t"
    "ds_read_b32 v8, v7\n\t"
    "s_waitcnt lgkmcnt(0)\n\t"
    "v_cmp_lt_u32 vcc, 0, v8\n\t"
    "s_and_b32 s44, vcc_hi, 0xf0000\n\t"
    "s_cbranch_scc0 L_cont56\n\t"
    "s_sleep 1\n\t"
    "s_sub_u32 s43, s43, 1\n\t"
    "s_cbranch_scc1 L_end\n\t"
    "s_branch L_slow56\n\t"
    "L_slow57:\n\t"
    "ds_read_b32 v8, v7\n\t"
    "s_waitcnt lgkmcnt(0)\n\t"
    "v_cmp_lt_u32 vcc, 0, v8\n\t"
    "s_and_b32 s44, vcc_hi, 0xf00000\n\t"
    "s_cbranch_scc0 L_cont57\n\t"
    "s_sleep 1\n\t"
    "s_sub_u32 s43, s43, 1\n\t"
    "s_cbranch_scc1 L_end\n\t"
    "s_branch L_slow57\n\t"
    "L_slow58:\n\t"
    "ds_read_b32 v8, v7\n\t"
    "s_waitcnt lgkmcnt(0)\n\t"
    "v_cmp_lt_u32 vcc, 3, v8\n\t"
    "s_and_b32 s44, vcc_lo, 0xfff\n\t"
    "s_cbranch_scc0 L_cont58\n\t"
    "s_sleep 1\n\t"
    "s_sub_u32 s43, s43, 1\n\t"
    "s_cbranch_scc1 L_end\n\t"
    "s_branch L_slow58\n\t"
    "L_poll60:\n\t"
    "s_sleep 1\n\t"
    "ds_read_b32 v8, v7\n\t"
    "s_sub_u32 s43, s43, 1\n\t"
    "s_cbranch_scc1 L_end\n\t"
    "s_waitcnt lgkmcnt(0)\n\t"
    "v_cmp_lt_u32 vcc, 0, v8\n\t"
    "s_and_b32 s44, vcc_lo, 0xf0000\n\t"
    "s_cbranch_scc1 L_poll60\n\t"
    "ds_read_b128 v[156:159], v124 offset:0\n\t"
    "ds_read_b128 v[164:167], v125 offset:0\n\t"
    "ds_read_b128 v[88:91], v124 offset:64\n\t"
    "ds_read_b128 v[92:95], v125 offset:64\n\t"
    "ds_read_b128 v[112:115], v124 offset:128\n\t"
    "ds_read_b128 v[116:119], v125 offset:128\n\t"
    "s_branch L_polld60\n\t"
    "L_poll61:\n\t"
    "s_sleep 1\n\t"
    "ds_read_b32 v8, v7\n\t"
    "s_sub_u32 s43, s43, 1\n\t"
    "s_cbranch_scc1 L_end\n\t"
    "s_waitcnt lgkmcnt(0)\n\t"
    "v_cmp_lt_u32 vcc, 0, v8\n\t"
    "s_and_b32 s44, vcc_lo, 0xf00000\n\t"
    "s_cbranch_scc1 L_poll61\n\t"
    "ds_read_b64_tr_b16 v[96:97], v5 offset:0\n\t"
    "ds_read_b64_tr_b16 v[98:99], v5 offset:768\n\t"
    "ds_read_b64_tr_b16 v[100:101], v6 offset:0\n\t"
    "ds_read_b64_tr_b16 v[102:103], v6 offset:768\n\t"
    "s_branch L_polld61\n\t"
    "L_poll62:\n\t"
    "s_sleep 1\n\t"
    "ds_read_b32 v8, v7 offset:256\n\t"
    "s_sub_u32 s43, s43, 1\n\t"
    "s_cbranch_scc1 L_end\n\t"
    "s_waitcnt lgkmcnt(0)\n\t"
    "v_cmp_lt_u32 vcc, 0, v8\n\t"
    "s_and_b32 s44, vcc_lo, s48\n\t"
    "s_cbranch_scc1 L_poll62\n\t"
    "ds_read_b128 v[128:131], v120 offset:192\n\t"
    "ds_read_b128 v[140:143], v121 offset:0\n\t"
    "ds_read_b128 v[132:135], v120 offset:256\n\t"
    "ds_read_b128 v[144:147], v121 offset:64\n\t"
    "ds_read_b128 v[136:139], v120 offset:320\n\t"
    "ds_read_b128 v[148:151], v121 offset:128\n\t"
    "s_branch L_polld62\n\t"
    "L_slow63:\n\t"
    "ds_read_b32 v8, v7\n\t"
    "s_waitcnt lgkmcnt(0)\n\t"
    "v_cmp_lt_u32 vcc, 0, v8\n\t"
    "s_and_b32 s44, vcc_lo, 0xf000000\n\t"
    "s_cbranch_scc0 L_cont63\n\t"
    "s_sleep 1\n\t"
    "s_sub_u32 s43, s43, 1\n\t"
    "s_cbranch_scc1 L_end\n\t"
    "s_branch L_slow63\n\t"
    "L_slow64:\n\t"
    "ds_read_b32 v8, v7\n\t"
    "s_waitcnt lgkmcnt(0)\n\t"
    "v_cmp_lt_u32 vcc, 0, v8\n\t"
    "s_and_b32 s44, vcc_lo, 0xf0000000\n\t"
    "s_cbranch_scc0 L_cont64\n\t"
    "s_sleep 1\n\t"
    "s_sub_u32 s43, s43, 1\n\t"
    "s_cbranch_scc1 L_end\n\t"
    "s_branch L_slow64\n\t"
    "L_slow65:\n\t"
    "ds_read_b32 v8, v7\n\t"
    "s_waitcnt lgkmcnt(0)\n\t"
    "v_cmp_lt_u32 vcc, 0, v8\n\t"
    "s_and_b32 s44, vcc_hi, 0xf\n\t"
    "s_cbranch_scc0 L_cont65\n\t"
    "s_sleep 1\n\t"
    "s_sub_u32 s43, s43, 1\n\t"
    "s_cbranch_scc1 L_end\n\t"
    "s_branch L_slow65\n\t"
    "L_slow66:\n\t"
    "ds_read_b32 v8, v7\n\t"
    "s_waitcnt lgkmcnt(0)\n\t"
    "v_cmp_lt_u32 vcc, 0, v8\n\t"
    "s_and_b32 s44, vcc_hi, s42\n\t"
    "s_cbranch_scc0 L_cont66\n\t"
    "s_sleep 1\n\t"
    "s_sub_u32 s43, s43, 1\n\t"
    "s_cbranch_scc1 L_end\n\t"
    "s_branch L_slow66\n\t"
    "L_slow67:\n\t"
    "ds_read_b32 v8, v7\n\t"
    "s_waitcnt lgkmcnt(0)\n\t"
    "v_cmp_lt_u32 vcc, 0, v8\n\t"
    "s_and_b32 s44, vcc_hi, 0xf0\n\t"
    "s_cbranch_scc0 L_cont67\n\t"
    "s_sleep 1\n\t"
    "s_sub_u32 s43, s43, 1\n\t"
    "s_cbranch_scc1 L_end\n\t"
    "s_branch L_slow67\n\t"
    "L_slow68:\n\t"
    "ds_read_b32 v8, v7\n\t"
    "s_waitcnt lgkmcnt(0)\n\t"
    "v_cmp_lt_u32 vcc, 0, v8\n\t"
    "s_and_b32 s44, vcc_hi, 0xf00\n\t"
    "s_cbranch_scc0 L_cont68\n\t"
    "s_sleep 1\n\t"
    "s_sub_u32 s43, s43, 1\n\t"
    "s_cbranch_scc1 L_end\n\t"
    "s_branch L_slow68\n\t"
    "L_slow69:\n\t"
    "ds_read_b32 v8, v7\n\t"
    "s_waitcnt lgkmcnt(0)\n\t"
    "v_cmp_lt_u32 vcc, 0, v8\n\t"
    "s_and_b32 s44, vcc_hi, 0xf000\n\t"
    "s_cbranch_scc0 L_cont69\n\t"
    "s_sleep 1\n\t"
    "s_sub_u32 s43, s43, 1\n\t"
    "s_cbranch_scc1 L_end\n\t"
    "s_branch L_slow69\n\t"
    "L_slow70:\n\t"
    "ds_read_b32 v8, v7\n\t"
    "s_waitcnt lgkmcnt(0)\n\t"
    "v_cmp_lt_u32 vcc, 0, v8\n\t"
    "s_and_b32 s44, vcc_hi, 0xf0000\n\t"
    "s_cbranch_scc0 L_cont70\n\t"
    "s_sleep 1\n\t"
    "s_sub_u32 s43, s43, 1\n\t"
    "s_cbranch_scc1 L_end\n\t"
    "s_branch L_slow70\n\t"
    "L_slow71:\n\t"
    "ds_read_b32 v8, v7\n\t"
    "s_waitcnt lgkmcnt(0)\n\t"
    "v_cmp_lt_u32 vcc, 0, v8\n\t"
    "s_and_b32 s44, vcc_hi, 0xf00000\n\t"
    "s_cbranch_scc0 L_cont71\n\t"
    "s_sleep 1\n\t"
    "s_sub_u32 s43, s43, 1\n\t"
    "s_cbranch_scc1 L_end\n\t"
    "s_branch L_slow71\n\t"
    "L_slow72:\n\t"
    "ds_read_b32 v8, v7\n\t"
    "s_waitcnt lgkmcnt(0)\n\t"
    "v_cmp_lt_u32 vcc, 2, v8\n\t"
    "s_and_b32 s44, vcc_lo, s38\n\t"
    "s_cbranch_scc0 L_cont72\n\t"
    "s_sleep 1\n\t"
    "s_sub_u32 s43, s43, 1\n\t"
    "s_cbranch_scc1 L_end\n\t"
    "s_branch L_slow72\n\t"
    "L_slow73:\n\t"
    "ds_read_b32 v8, v7\n\t"
    "s_waitcnt lgkmcnt(0)\n\t"
    "v_cmp_lt_u32 vcc, 1, v8\n\t"
    "s_and_b32 s44, vcc_lo, s38\n\t"
    "s_cbranch_scc0 L_cont73\n\t"
    "s_sleep 1\n\t"
    "s_sub_u32 s43, s43, 1\n\t"
    "s_cbranch_scc1 L_end\n\t"
    "s_branch L_slow73\n\t"
    "L_slow74:\n\t"
    "ds_read_b32 v8, v7\n\t"
    "s_waitcnt lgkmcnt(0)\n\t"
    "v_cmp_lt_u32 vcc, 0, v8\n\t"
    "s_and_b32 s44, vcc_lo, s38\n\t"
    "s_cbranch_scc0 L_cont74\n\t"
    "s_sleep 1\n\t"
    "s_sub_u32 s43, s43, 1\n\t"
    "s_cbranch_scc1 L_end\n\t"
    "s_branch L_slow74\n\t"
    "L_end:\n\t"
    : "+v"(tid)
    : "s"(__builtin_amdgcn_kernarg_segment_ptr()), "s"(blockIdx.x), "s"((unsigned)(size_t)smem)
    : "v1", "v2", "v3", "v4", "v5", "v6", "v7", "v8", "v9", "v10", "v11", "v12", "v13", "v14", "v15", "v16", "v17", "v18", "v19", "v20", "v21", "v22", "v23", "v24", "v25", "v26", "v27", "v28", "v29", "v30", "v31", "v32", "v33", "v34", "v35", "v36", "v37", "v38", "v39", "v40", "v41", "v42", "v43", "v44", "v45", "v46", "v47", "v48", "v49", "v50", "v51", "v52", "v53", "v54", "v55", "v56", "v57", "v58", "v59", "v60", "v61", "v62", "v63", "v64", "v65", "v66", "v67", "v68", "v69", "v70", "v71", "v72", "v73", "v74", "v75", "v76", "v77", "v78", "v79", "v80", "v81", "v82", "v83", "v84", "v85", "v86", "v87", "v88", "v89", "v90", "v91", "v92", "v93", "v94", "v95", "v96", "v97", "v98", "v99", "v100", "v101", "v102", "v103", "v104", "v105", "v106", "v107", "v108", "v109", "v110", "v111", "v112", "v113", "v114", "v115", "v116", "v117", "v118", "v119", "v120", "v121", "v122", "v123", "v124", "v125", "v126", "v127", "v128", "v129", "v130", "v131", "v132", "v133", "v134", "v135", "v136", "v137", "v138", "v139", "v140", "v141", "v142", "v143", "v144", "v145", "v146", "v147", "v148", "v149", "v150", "v151", "v152", "v153", "v154", "v155", "v156", "v157", "v158", "v159", "v160", "v161", "v162", "v163", "v164", "v165", "v166", "v167", "s4", "s5", "s6", "s7", "s8", "s9", "s10", "s11", "s12", "s13", "s14", "s15", "s16", "s17", "s18", "s19", "s20", "s21", "s22", "s23", "s24", "s25", "s26", "s27", "s28", "s29", "s30", "s31", "s33", "s34", "s35", "s36", "s37", "s38", "s39", "s40", "s41", "s42", "s43", "s44", "s45", "s46", "s47", "s48", "s49", "vcc", "scc", "memory");
}

extern "C" void kernel_launch(void* const* d_in, const int* in_sizes, int n_in,
                              void* d_out, int out_size, void* d_ws, size_t ws_size,
                              hipStream_t stream) {
  fpn_fused<<<256, 768, 0, stream>>>((const __hip_bfloat16*)d_in[0], (const __hip_bfloat16*)d_in[1],
                                     (const __hip_bfloat16*)d_in[2], (const __hip_bfloat16*)d_in[3],
                                     (__hip_bfloat16*)d_out);
}
```
